```python
import torch

def _softmax(x, axis=-1):
    return torch.softmax(x, dim=axis)
def _gelu(x):
    return torch.nn.functional.gelu(x, approximate="tanh")
def _silu(x):
    return torch.nn.functional.silu(x)
def _sigmoid(x):
    return torch.sigmoid(x)
def _layernorm(x, axis=-1, eps=1e-5):
    m = x.mean(dim=axis, keepdim=True)
    v = x.var(dim=axis, keepdim=True, unbiased=False)
    return (x - m) / torch.sqrt(v + eps)
def _scatter_add(tgt, idx, src, axis=0):
    return tgt.clone().index_add_(axis, idx, src)
def _segment_sum(src, seg, n):
    out = torch.zeros((n,) + src.shape[1:], dtype=src.dtype)
    return out.index_add_(0, seg, src)
def _pad(x, pw):
    flat = tuple(v for pair in reversed(pw) for v in pair)
    return torch.nn.functional.pad(x, flat)

kernel_name = 'sequential_preLN__mha_block_cached__glu_gated_glu__decode'

def setup_inputs():

    _w = (lambda a: (lambda t: t.to(torch.bfloat16) if t.is_floating_point() else t)(torch.as_tensor(a)))
    def _A_setup():
        B, Sq, D = 4, 1, 512
        H, Sk, Dh = 32, 8192, 128
        return {
            "x": _w((lambda *s: torch.randn(*s, dtype=torch.bfloat16) * 0.05)(B, Sq, D)),
            "k_cache": _w((lambda *s: torch.randn(*s, dtype=torch.bfloat16) * 3.2)(B, H, Sk, Dh)),
            "v_cache": _w((lambda *s: torch.randn(*s, dtype=torch.bfloat16) * 0.4)(B, H, Sk, Dh)),
            "wq": _w((lambda *s: torch.randn(*s, dtype=torch.bfloat16) * 3.2)(D, H * Dh) * (D ** -0.5)),
            "wo": _w((lambda *s: torch.randn(*s, dtype=torch.bfloat16) * 0.4)(H * Dh, D) * ((H * Dh) ** -0.5)),
        }
    _A_in = _A_setup()

    def _B_setup():
        B, S, D, F = 4, 1, 512, 1792
        return {
            "x": _w((lambda *s: torch.randn(*s, dtype=torch.bfloat16) * 0.05)(B, S, D)),
            "w_gate": _w((lambda *s: torch.randn(*s, dtype=torch.bfloat16) * 0.1)(D, F)),
            "w_up": _w((lambda *s: torch.randn(*s, dtype=torch.bfloat16) * 0.05)(D, F)),
            "w_down": _w((lambda *s: torch.randn(*s, dtype=torch.bfloat16) * 0.05)(F, D)),
        }
    _B_in = _B_setup()

    return {
        "A_x": _A_in["x"],
        "A_k_cache": _A_in["k_cache"],
        "A_v_cache": _A_in["v_cache"],
        "A_wq": _A_in["wq"],
        "A_wo": _A_in["wo"],
        "B_x": _B_in["x"],
        "B_w_gate": _B_in["w_gate"],
        "B_w_up": _B_in["w_up"],
        "B_w_down": _B_in["w_down"],
    }

def reference(A_x, A_k_cache, A_v_cache, A_wq, A_wo, B_x, B_w_gate, B_w_up, B_w_down):
    def _A_ref(x, k_cache, v_cache, wq, wo):
        B, Sq, D = x.shape
        H, Dh = 32, 128
        q = (lambda x, *d: x.permute(*d))((x @ wq).reshape(B, Sq, H, Dh), 0, 2, 1, 3)
        scores = torch.einsum("bhqd,bhkd->bhqk", q, k_cache) * (Dh ** -0.5)
        p = _softmax(scores, axis=-1)
        o = torch.einsum("bhqk,bhkd->bhqd", p, v_cache)
        return (lambda x, *d: x.permute(*d))(o, 0, 2, 1, 3).reshape(B, Sq, H * Dh) @ wo

    def _B_ref(x, w_gate, w_up, w_down):
        g = torch.einsum("bsd,df->bsf", x, w_gate)
        u = torch.einsum("bsd,df->bsf", x, w_up)
        h = _sigmoid(g) * u
        return torch.einsum("bsf,fd->bsd", h, w_down)

    x = A_x
    h = x + _A_ref(_layernorm(x, axis=-1), A_k_cache, A_v_cache, A_wq, A_wo)
    h2 = h + _B_ref(_layernorm(h, axis=-1), B_w_gate, B_w_up, B_w_down)
    return h2
```

```cpp
#include <hip/hip_runtime.h>
#include <hip/hip_bf16.h>
#include <cstdint>

typedef unsigned short u16;
typedef unsigned v4u __attribute__((ext_vector_type(4)));
typedef float f32x4 __attribute__((ext_vector_type(4)));
typedef __bf16 bf16x2 __attribute__((ext_vector_type(2)));

#define MIMIC 0

constexpr int B = 4, D = 512, H = 32, SK = 8192, DH = 128, F = 1792;

__device__ __forceinline__ float bf2f(u16 b) { return __builtin_bit_cast(float, (unsigned)b << 16); }
__device__ __forceinline__ float rbf(float f) {
  __bf16 h = (__bf16)f; return (float)h;
}
__device__ __forceinline__ u16 f2bf(float f) { __bf16 h = (__bf16)f; return __builtin_bit_cast(u16, h); }
__device__ __forceinline__ float mr(float f) { return MIMIC ? rbf(f) : f; }
__device__ __forceinline__ float dot2(unsigned k, unsigned q, float acc) {
  return __builtin_amdgcn_fdot2_f32_bf16(__builtin_bit_cast(bf16x2, k), __builtin_bit_cast(bf16x2, q), acc, false);
}
__device__ __forceinline__ float wave_sum(float v) {
  for (int o = 32; o > 0; o >>= 1) v += __shfl_xor(v, o);
  return v;
}
__device__ __forceinline__ float wave_max(float v) {
  for (int o = 32; o > 0; o >>= 1) v = fmaxf(v, __shfl_xor(v, o));
  return v;
}
__device__ __forceinline__ void ln_row(float (&x)[8]) {
  float s = 0.f;
  for (int j = 0; j < 8; ++j) s += x[j];
  s = wave_sum(s);
  const float mean = s * (1.f / 512.f);
  float vs = 0.f;
  for (int j = 0; j < 8; ++j) { float d = x[j] - mean; vs += d * d; }
  vs = wave_sum(vs);
  const float m = mr(mean), v = mr(vs * (1.f / 512.f));
  const float sd = mr(sqrtf(mr(v + 1e-5f)));
  for (int j = 0; j < 8; ++j) x[j] = rbf(mr(x[j] - m) / sd);
}

__global__ void __launch_bounds__(256) k_q(const u16* x, const u16* wq, u16* q) {
  __shared__ float xn[B][D];
  __shared__ float red[4][B][64];
  const int tid = threadIdx.x, lane = tid & 63, w = tid >> 6;
  { float v[8];
    for (int j = 0; j < 8; ++j) v[j] = bf2f(x[w * D + lane * 8 + j]);
    ln_row(v);
    for (int j = 0; j < 8; ++j) xn[w][lane * 8 + j] = v[j]; }
  __syncthreads();
  const int col = blockIdx.x * 64 + lane;
  float acc[B] = {0.f, 0.f, 0.f, 0.f};
  for (int k = w * 128; k < w * 128 + 128; ++k) {
    const float wv = bf2f(wq[(size_t)k * (H * DH) + col]);
    for (int b = 0; b < B; ++b) acc[b] = fmaf(xn[b][k], wv, acc[b]);
  }
  for (int b = 0; b < B; ++b) red[w][b][lane] = acc[b];
  __syncthreads();
  { const int b = w; float s = red[0][b][lane] + red[1][b][lane] + red[2][b][lane] + red[3][b][lane];
    q[b * (H * DH) + col] = f2bf(s); }
}

template <int CTRL> __device__ __forceinline__ float dpp(float x) {
  return __builtin_bit_cast(float, __builtin_amdgcn_mov_dpp(__builtin_bit_cast(int, x), CTRL, 0xf, 0xf, true));
}
constexpr int XOR1 = 0xB1, XOR2 = 0x4E, XOR7 = 0x141, XOR8 = 0x128;

constexpr int NSPLIT = 2;
__global__ void __launch_bounds__(512) k_attn(const u16* kc, const u16* vc, const u16* q, float* part) {
  __shared__ float mrg[8][132];
  const int tid = threadIdx.x, lane = tid & 63, w = tid >> 6, r = lane >> 4, c = lane & 15;
  const int bh = blockIdx.x >> 1, half = blockIdx.x & 1;
  const v4u qp = *(const v4u*)(q + bh * DH + c * 8);
  const size_t base = ((size_t)bh * SK + half * (SK / 2)) * DH;
  float m = -INFINITY, l = 0.f, acc[8] = {0, 0, 0, 0, 0, 0, 0, 0};
  for (int ch = w; ch < 64; ch += 8) {
    const u16* kp = kc + base + (size_t)ch * 64 * DH + lane * 8;
    const u16* vp = vc + base + (size_t)ch * 64 * DH + lane * 8;
    v4u K[16], V[16];
#pragma unroll
    for (int i = 0; i < 16; ++i) K[i] = __builtin_nontemporal_load((const v4u*)(kp + i * 512));
#pragma unroll
    for (int i = 0; i < 16; ++i) V[i] = __builtin_nontemporal_load((const v4u*)(vp + i * 512));
    float x[16];
#pragma unroll
    for (int i = 0; i < 16; ++i) { float d = 0.f;
      d = dot2(K[i][0], qp[0], d); d = dot2(K[i][1], qp[1], d); d = dot2(K[i][2], qp[2], d); d = dot2(K[i][3], qp[3], d); x[i] = d; }
#define TR_STEP(HALF, CTRL, BIT) _Pragma("unroll") for (int h = 0; h < (HALF); ++h) { \
      const float lo_ = x[h], hi_ = x[h + (HALF)]; const float t1_ = lo_ + dpp<CTRL>(lo_), t2_ = hi_ + dpp<CTRL>(hi_); \
      x[h] = (c & (BIT)) ? t2_ : t1_; }
    TR_STEP(8, XOR8, 8) TR_STEP(4, XOR7, 4) TR_STEP(2, XOR2, 2) TR_STEP(1, XOR1, 1)
#undef TR_STEP
    const float s = mr(mr(x[0]) * 0.08838834764831845f);
    const float mn = fmaxf(m, wave_max(s));
    const float alpha = __expf(m - mn), p = __expf(s - mn);
    l = l * alpha + p; m = mn;
#pragma unroll
    for (int k = 0; k < 8; ++k) acc[k] *= alpha;
#pragma unroll
    for (int i = 0; i < 16; ++i) {
      const float pi = __shfl(p, (lane & 48) + i);
#pragma unroll
      for (int k = 0; k < 4; ++k) {
        acc[2 * k] = fmaf(pi, __builtin_bit_cast(float, V[i][k] << 16), acc[2 * k]);
        acc[2 * k + 1] = fmaf(pi, __builtin_bit_cast(float, V[i][k] & 0xffff0000u), acc[2 * k + 1]);
      }
    }
  }
  l = wave_sum(l);
#pragma unroll
  for (int k = 0; k < 8; ++k) { acc[k] += __shfl_xor(acc[k], 16); acc[k] += __shfl_xor(acc[k], 32); }
  if (r == 0) { for (int k = 0; k < 8; ++k) mrg[w][4 + 8 * c + k] = acc[k]; if (c == 0) { mrg[w][0] = m; mrg[w][1] = l; } }
  __syncthreads();
  if (tid < 128) {
    float M = -INFINITY; for (int i = 0; i < 8; ++i) M = fmaxf(M, mrg[i][0]);
    float L = 0.f, o = 0.f;
    for (int i = 0; i < 8; ++i) { const float wt = __expf(mrg[i][0] - M); L += wt * mrg[i][1]; o += wt * mrg[i][4 + tid]; }
    float* pp = part + (size_t)blockIdx.x * 132;
    pp[4 + tid] = o; if (tid == 0) { pp[0] = M; pp[1] = L; }
  }
}

__global__ void __launch_bounds__(256) k_wo(const float* part, const u16* wo, float* y) {
  __shared__ float o[B][DH];
  const int tid = threadIdx.x, h = blockIdx.x;
  for (int i = tid; i < B * DH; i += 256) { const int b = i >> 7, d = i & 127;
    const float* p0 = part + (size_t)((b * H + h) * 2) * 132; const float* p1 = p0 + 132;
    const float M = fmaxf(p0[0], p1[0]), w0 = __expf(p0[0] - M), w1 = __expf(p1[0] - M);
    o[b][d] = mr((w0 * p0[4 + d] + w1 * p1[4 + d]) / (w0 * p0[1] + w1 * p1[1])); }
  __syncthreads();
  float acc[B][2] = {};
  for (int k = 0; k < DH; ++k) { const unsigned wv = *(const unsigned*)(wo + (size_t)(h * DH + k) * D + tid * 2);
    const float w0 = __builtin_bit_cast(float, wv << 16), w1 = __builtin_bit_cast(float, wv & 0xffff0000u);
    for (int b = 0; b < B; ++b) { acc[b][0] = fmaf(o[b][k], w0, acc[b][0]); acc[b][1] = fmaf(o[b][k], w1, acc[b][1]); } }
  for (int b = 0; b < B; ++b) { y[(size_t)(h * B + b) * D + tid * 2] = acc[b][0]; y[(size_t)(h * B + b) * D + tid * 2 + 1] = acc[b][1]; }
}

__global__ void __launch_bounds__(256) k_gu(const u16* x, const float* y, const u16* wg, const u16* wu, float* hres, float* hh) {
  __shared__ float hn[B][D];
  __shared__ float red[4][B][2][64];
  const int tid = threadIdx.x, lane = tid & 63, w = tid >> 6;
  { float v[8];
    for (int j = 0; j < 8; ++j) { float s = 0.f; for (int h = 0; h < H; ++h) s += y[(size_t)(h * B + w) * D + lane * 8 + j];
      v[j] = rbf(bf2f(x[w * D + lane * 8 + j]) + mr(s)); if (blockIdx.x == 0) hres[w * D + lane * 8 + j] = v[j]; }
    ln_row(v);
    for (int j = 0; j < 8; ++j) hn[w][lane * 8 + j] = v[j]; }
  __syncthreads();
  const int col = blockIdx.x * 64 + lane;
  float ag[B] = {}, au[B] = {};
  for (int k = w * 128; k < w * 128 + 128; ++k) {
    const float g = bf2f(wg[(size_t)k * F + col]), u = bf2f(wu[(size_t)k * F + col]);
    for (int b = 0; b < B; ++b) { ag[b] = fmaf(hn[b][k], g, ag[b]); au[b] = fmaf(hn[b][k], u, au[b]); }
  }
  for (int b = 0; b < B; ++b) { red[w][b][0][lane] = ag[b]; red[w][b][1][lane] = au[b]; }
  __syncthreads();
  { const int b = w; float g = 0.f, u = 0.f; for (int i = 0; i < 4; ++i) { g += red[i][b][0][lane]; u += red[i][b][1][lane]; }
    g = mr(g); u = mr(u);
    const float sg = mr(1.f / (1.f + __expf(-g)));
    hh[b * F + col] = mr(sg * u); }
}

__global__ void __launch_bounds__(256) k_down(const float* hh, const u16* wd, const float* hres, u16* out) {
  __shared__ float hs[B][F];
  __shared__ float red[4][B][64];
  const int tid = threadIdx.x, lane = tid & 63, w = tid >> 6;
  for (int i = tid; i < B * F; i += 256) hs[i / F][i % F] = hh[i];
  __syncthreads();
  const int col = blockIdx.x * 64 + lane;
  float acc[B] = {};
  for (int k = w * 448; k < w * 448 + 448; ++k) { const float wv = bf2f(wd[(size_t)k * D + col]);
    for (int b = 0; b < B; ++b) acc[b] = fmaf(hs[b][k], wv, acc[b]); }
  for (int b = 0; b < B; ++b) red[w][b][lane] = acc[b];
  __syncthreads();
  { const int b = w; const float s = red[0][b][lane] + red[1][b][lane] + red[2][b][lane] + red[3][b][lane];
    out[b * D + col] = f2bf(hres[b * D + col] + mr(s)); }
}

extern "C" void kernel_launch(void* const* d_in, const int* in_sizes, int n_in, void* d_out, int out_size, void* d_ws, size_t ws_size, hipStream_t stream) {
  const u16* x = (const u16*)d_in[0]; const u16* kc = (const u16*)d_in[1]; const u16* vc = (const u16*)d_in[2];
  const u16* wq = (const u16*)d_in[3]; const u16* wo = (const u16*)d_in[4];
  const u16* wg = (const u16*)d_in[6]; const u16* wu = (const u16*)d_in[7]; const u16* wd = (const u16*)d_in[8];
  char* ws = (char*)d_ws;
  u16* q = (u16*)ws; float* part = (float*)(ws + 65536); float* y = (float*)(ws + 65536 + 262144);
  float* hres = (float*)(ws + 65536 + 262144 + 524288); float* hh = hres + 4096;
  k_q<<<64, 256, 0, stream>>>(x, wq, q);
  k_attn<<<256, 512, 0, stream>>>(kc, vc, q, part);
  k_wo<<<32, 256, 0, stream>>>(part, wo, y);
  k_gu<<<28, 256, 0, stream>>>(x, y, wg, wu, hres, hh);
  k_down<<<8, 256, 0, stream>>>(hh, wd, hres, (u16*)d_out);
}
```

```cpp
#include <hip/hip_runtime.h>
#include <hip/hip_bf16.h>
#include <cstdint>

typedef unsigned short u16;
typedef unsigned v4u __attribute__((ext_vector_type(4)));
typedef unsigned v2u __attribute__((ext_vector_type(2)));
typedef float v4f __attribute__((ext_vector_type(4)));
typedef float v2f __attribute__((ext_vector_type(2)));
typedef __bf16 bf16x2 __attribute__((ext_vector_type(2)));
typedef __bf16 bf16x8 __attribute__((ext_vector_type(8)));
#define LAS __attribute__((address_space(3)))

#define PRE 3
#define HREP 16
#define WAUX 2
#define ROTJ 0
#define ROTB 0
#define DELAY 0
#define NOFFN 0
#define FAUX 2
#define OAUX 2
#define NOWQ 0
#define NOWO 0
#define PACE 8
#define RPAR 0
#define ROTO 0
#define ORND 1
#define MAP 0
#define DLT 0
#define FMODE 0
#define OSLEEP 0
#define SLEEPN 1

constexpr int D = 512, F = 1792;
constexpr int NIT = 64;
constexpr int WS_S = 4096;
constexpr int WS_H = 65536;
constexpr int WS_HH = 262144;
constexpr int WS_Z = 1048576;
constexpr int WS_Q = 2097152;
constexpr int WS_BYTES = 3145728;
#define NOZ 0
#define NOQ 0
constexpr int SPIN_CAP = NOZ ? 1 : 400000;
constexpr int SPIN_CAPQ = NOQ ? 1 : 400000;
constexpr int L_FR = 0;
constexpr int FRT = 16384 + 16;
constexpr int L_QP = 8 * FRT;
constexpr int L_PART = L_QP + 256;
constexpr int L_ML = L_PART + 2048;
constexpr int L_TAG = L_ML + 32;
constexpr int L_HN = L_TAG + 32;
constexpr int L_RED = L_HN + 4352;
constexpr int L_END = L_RED + 1792;

constexpr float L2E = 1.4426950408889634f;

__device__ __forceinline__ float asf(unsigned u) { return __builtin_bit_cast(float, u); }
__device__ __forceinline__ unsigned asu(float f) { return __builtin_bit_cast(unsigned, f); }
__device__ __forceinline__ float blo(unsigned u) { return asf(u << 16); }
__device__ __forceinline__ float bhi(unsigned u) { return asf(u & 0xffff0000u); }
__device__ __forceinline__ unsigned pk(float a, float b) {
  bf16x2 r; r[0] = (__bf16)a; r[1] = (__bf16)b; return __builtin_bit_cast(unsigned, r);
}
__device__ __forceinline__ float rbf(float f) { __bf16 h = (__bf16)f; return (float)h; }
__device__ __forceinline__ float dot2(unsigned a, unsigned b, float acc) {
  return __builtin_amdgcn_fdot2_f32_bf16(__builtin_bit_cast(bf16x2, a), __builtin_bit_cast(bf16x2, b), acc, false);
}
template <int CTRL> __device__ __forceinline__ unsigned dppu(unsigned x) {
  return (unsigned)__builtin_amdgcn_mov_dpp((int)x, CTRL, 0xf, 0xf, true);
}
template <int CTRL> __device__ __forceinline__ float dpp(float x) { return asf(dppu<CTRL>(asu(x))); }
constexpr int XOR1 = 0xB1, XOR2 = 0x4E, HMIR = 0x141, ROR8 = 0x128, BCAST = 0x150, SHL = 0x100;
__device__ __forceinline__ float row_sum(float x) {
  x += dpp<XOR1>(x); x += dpp<XOR2>(x); x += dpp<HMIR>(x); x += dpp<ROR8>(x); return x;
}
__device__ __forceinline__ float xrow_sum(float x) {
  auto s = __builtin_amdgcn_permlane16_swap(asu(x), asu(x), false, false); x = asf(s[0]) + asf(s[1]);
  auto t = __builtin_amdgcn_permlane32_swap(asu(x), asu(x), false, false); return asf(t[0]) + asf(t[1]);
}
__device__ __forceinline__ float xrow_max(float x) {
  auto s = __builtin_amdgcn_permlane16_swap(asu(x), asu(x), false, false); x = fmaxf(asf(s[0]), asf(s[1]));
  auto t = __builtin_amdgcn_permlane32_swap(asu(x), asu(x), false, false); return fmaxf(asf(t[0]), asf(t[1]));
}
__device__ __forceinline__ v2f tr_reduce(const float (&a)[8]) {
  float b[4];
#pragma unroll
  for (int j = 0; j < 4; ++j) { auto s = __builtin_amdgcn_permlane32_swap(asu(a[j]), asu(a[j + 4]), false, false); b[j] = asf(s[0]) + asf(s[1]); }
  v2f c;
#pragma unroll
  for (int j = 0; j < 2; ++j) { auto s = __builtin_amdgcn_permlane16_swap(asu(b[j]), asu(b[j + 2]), false, false); c[j] = asf(s[0]) + asf(s[1]); }
  return c;
}
__device__ __forceinline__ v2f ln_fin(float mean, float vs) {
  const float m = rbf(mean), v = rbf(vs * (1.f / 512.f));
  const float sd = rbf(__builtin_amdgcn_sqrtf(rbf(v + 1e-5f)));
  return v2f{m, __builtin_amdgcn_rcpf(sd)};
}
__device__ __forceinline__ v2f ln_stats(v4u in) {
  float x[8];
#pragma unroll
  for (int j = 0; j < 4; ++j) { x[2 * j] = blo(in[j]); x[2 * j + 1] = bhi(in[j]); }
  float s = ((x[0] + x[1]) + (x[2] + x[3])) + ((x[4] + x[5]) + (x[6] + x[7]));
  s = xrow_sum(row_sum(s));
  const float mean = s * (1.f / 512.f);
  float v0 = 0.f, v1 = 0.f;
#pragma unroll
  for (int j = 0; j < 4; ++j) { const float d0 = x[2 * j] - mean, d1 = x[2 * j + 1] - mean; v0 = fmaf(d0, d0, v0); v1 = fmaf(d1, d1, v1); }
  return ln_fin(mean, xrow_sum(row_sum(v0 + v1)));
}
__device__ __forceinline__ unsigned ln_apply(unsigned in, v2f st) {
  const unsigned d = pk(blo(in) - st[0], bhi(in) - st[0]); return pk(blo(d) * st[1], bhi(d) * st[1]);
}
__device__ __forceinline__ void bar() { asm volatile("s_waitcnt lgkmcnt(0)\n\ts_barrier" ::: "memory"); }
#define SB() __builtin_amdgcn_sched_barrier(0)
#define RSRC(p, n) __builtin_amdgcn_make_buffer_rsrc((void*)(p), 0, (int)(n), 0x00020000)
#define LD128(r, vo, so, aux) __builtin_amdgcn_raw_buffer_load_b128(r, vo, so, aux)

#define STAMP(i) do {} while (0)
#define TNOW() 0u
#define STAMPV(i, t_) do {} while (0)

template <bool RE, bool LAST>
__device__ __forceinline__ void proc(v4u (&a)[8], const v4u q, float& m, float& l, float (&acc)[8],
                                     const __amdgpu_buffer_rsrc_t rk, const __amdgpu_buffer_rsrc_t rv, int vo, int so) {
  float x[4];
#pragma unroll
  for (int i = 0; i < 4; ++i) {
    x[i] = dot2(a[i][3], q[3], dot2(a[i][2], q[2], dot2(a[i][1], q[1], dot2(a[i][0], q[0], 0.f))));
    asm volatile("" : "+v"(x[i]));
    SB();
    if (RE) a[i] = LD128(rk, vo + i * 1024, so, 2);
    SB();
  }
#pragma unroll
  for (int i = 0; i < 4; ++i) x[i] = row_sum(x[i]);
  const unsigned r01 = pk(x[0], x[1]), r23 = pk(x[2], x[3]);
  const float SC = 0.08838834764831845f;
  const unsigned t01 = pk(blo(r01) * SC, bhi(r01) * SC), t23 = pk(blo(r23) * SC, bhi(r23) * SC);
  const float s0 = blo(t01), s1 = bhi(t01), s2 = blo(t23), s3 = bhi(t23);
  float mn = fmaxf(fmaxf(fmaxf(s0, s1), fmaxf(s2, s3)), m);
  if (LAST) mn = xrow_max(mn);
  const float mL = mn * L2E;
  const float alpha = __builtin_amdgcn_exp2f((m - mn) * L2E);
  float p[4];
  p[0] = __builtin_amdgcn_exp2f(fmaf(s0, L2E, -mL)); p[1] = __builtin_amdgcn_exp2f(fmaf(s1, L2E, -mL));
  p[2] = __builtin_amdgcn_exp2f(fmaf(s2, L2E, -mL)); p[3] = __builtin_amdgcn_exp2f(fmaf(s3, L2E, -mL));
  l = fmaf(l, alpha, (p[0] + p[1]) + (p[2] + p[3]));
  if (LAST) l = xrow_sum(l);
  m = mn;
#pragma unroll
  for (int k = 0; k < 8; ++k) acc[k] *= alpha;
  SB();
#pragma unroll
  for (int i = 0; i < 4; ++i) {
#pragma unroll
    for (int k = 0; k < 4; ++k) { acc[2 * k] = fmaf(p[i], blo(a[4 + i][k]), acc[2 * k]); acc[2 * k + 1] = fmaf(p[i], bhi(a[4 + i][k]), acc[2 * k + 1]); }
    asm volatile("" : "+v"(acc[0]), "+v"(acc[1]), "+v"(acc[2]), "+v"(acc[3]), "+v"(acc[4]), "+v"(acc[5]), "+v"(acc[6]), "+v"(acc[7]));
    SB();
    if (RE) a[4 + i] = LD128(rv, vo + i * 1024, so, 2);
    SB();
  }
}

template <int PC>
__device__ __forceinline__ void stage_gu(LAS char* sm, const u16* wg, const u16* wu, int fg, int ww, int lane) {
  const int mat = ww & 1, oc = lane & 7, rho = lane >> 3;
  const __amdgpu_buffer_rsrc_t rg = RSRC(mat ? wu : wg, D * F * 2);
  const int go = (rho * 2 * F + oc * 8) * 2, gs = ((ww >> 1) * 128 * F + fg * 64) * 2;
  v4u t[16];
#pragma unroll
  for (int j = 0; j < 16; ++j) {
    if (PC && j >= PC) { SB(); asm volatile("s_waitcnt vmcnt(%0)" :: "n"(PC > 0 ? PC - 1 : 0)); SB(); }
    t[j] = NOFFN ? v4u{0, 0, 0, 0} : LD128(rg, go, gs + ((j >> 1) * 16 + (j & 1)) * F * 2, FAUX);
  }
#pragma unroll
  for (int rd = 0; rd < 2; ++rd) {
    const int kap = (ww >> 1) * 16 + rd * 8 + rho;
    LAS char* dst = sm + L_FR + oc * FRT + ((kap >> 2) * 64 + (kap & 3) * 16 + mat) * 16;
#pragma unroll
    for (int f = 0; f < 8; ++f) {
      const unsigned sel = (f & 1) ? 0x07060302u : 0x05040100u;
      v4u o;
#pragma unroll
      for (int j = 0; j < 4; ++j) o[j] = __builtin_amdgcn_perm(t[rd * 8 + 2 * j + 1][f >> 1], t[rd * 8 + 2 * j][f >> 1], sel);
      *(LAS v4u*)(dst + f * 32) = o;
    }
  }
}
template <int PC>
__device__ __forceinline__ void stage_dn(LAS char* sm, const u16* wd, int cgD, int ww, int lane) {
  const int kap = lane >> 1, oc = lane & 1;
  const __amdgpu_buffer_rsrc_t rg = RSRC(wd, D * F * 2);
  const int go = (kap * 8 * D + oc * 8) * 2, gs = (ww * 256 * D + cgD * 16) * 2;
  v4u t[8];
#pragma unroll
  for (int j = 0; j < 8; ++j) {
    if (PC && j >= PC) { SB(); asm volatile("s_waitcnt vmcnt(%0)" :: "n"(PC > 0 ? PC - 1 : 0)); SB(); }
    t[j] = NOFFN ? v4u{0, 0, 0, 0} : LD128(rg, go, gs + j * D * 2, FAUX);
  }
  LAS char* dst = sm + L_FR + ww * 8704 + (kap >> 2) * 1088 + (kap & 3) * 272 + oc * 128;
#pragma unroll
  for (int f = 0; f < 8; ++f) {
    const unsigned sel = (f & 1) ? 0x07060302u : 0x05040100u;
    v4u o;
#pragma unroll
    for (int j = 0; j < 4; ++j) o[j] = __builtin_amdgcn_perm(t[2 * j + 1][f >> 1], t[2 * j][f >> 1], sel);
    *(LAS v4u*)(dst + f * 16) = o;
  }
}

__global__ void __launch_bounds__(512, 2)
fused_block(const u16* __restrict__ x, const u16* __restrict__ kc, const u16* __restrict__ vc, const u16* __restrict__ wq,
            const u16* __restrict__ wo, const u16* __restrict__ wg, const u16* __restrict__ wu, const u16* __restrict__ wd,
            u16* out, char* ws) {
  __shared__ __attribute__((aligned(16))) char smem_[L_END];
  LAS char* const sm = (LAS char*)smem_;
  const int tid = threadIdx.x, lane = tid & 63, w = __builtin_amdgcn_readfirstlane(tid >> 6);
  const int r = lane >> 4, c = lane & 15;
  const int bid = blockIdx.x;
  const int xc = bid & 7, slot = bid >> 3, side = slot & 1, pr = slot >> 1;
  const int b = pr >> 2, bh = pr * 8 + ((xc + ROTJ * (pr & 3) + ROTB * (b >> 1)) & 7), h = bh & 31;
  const bool rsel = side == 1 && (xc & 1) == RPAR;
  const bool isGU = rsel && pr < 7, isDN = rsel && pr >= 7 && pr < 15;
  const int fg = (xc >> 1) * 7 + pr, cgD = (xc >> 1) * 8 + pr - 7;
  const int jj = b * 2 + side;
  const __amdgpu_buffer_rsrc_t rws = RSRC(ws, WS_BYTES);
  unsigned tag;
  STAMP(0);

  if (w >= 4) {
    const int sw = w - 4;
    const __amdgpu_buffer_rsrc_t rk = RSRC(kc, 0x10000000), rv = RSRC(vc, 0x10000000);
    const int vo = lane * 16;
    int so = bh * (2 << 20) + (side * 4 + sw) * 4096;
    v4u A[8], P[PRE > 0 ? PRE : 1][8];
    if (DELAY) __builtin_amdgcn_s_sleep(DELAY);
#pragma unroll
    for (int i = 0; i < 4; ++i) A[i] = LD128(rk, vo + i * 1024, so, 2);
#pragma unroll
    for (int i = 0; i < 4; ++i) A[4 + i] = LD128(rv, vo + i * 1024, so, 2);
#pragma unroll
    for (int u = 0; u < PRE; ++u) {
#pragma unroll
      for (int i = 0; i < 8; ++i) {
        if (PACE) { SB(); asm volatile("s_waitcnt vmcnt(%0)" :: "n"(PACE - 1)); SB(); }
        P[u][i] = LD128(i < 4 ? rk : rv, vo + (i & 3) * 1024, so + (u + 1) * 32768, 2);
      }
    }
    SB();
    const unsigned t6 = TNOW();
    bar();
    STAMP(1); STAMPV(6, t6);
    const v4u q = *(LAS v4u*)(sm + L_QP + c * 16);
    tag = *(LAS unsigned*)(sm + L_TAG);
    float m = -3.0e38f, l = 0.f, acc[8] = {0.f, 0.f, 0.f, 0.f, 0.f, 0.f, 0.f, 0.f};
    so += (PRE + 1) * 32768;
    const int nit = side ? NIT - DLT : NIT + DLT;
    proc<true, false>(A, q, m, l, acc, rk, rv, vo, so);
#pragma unroll
    for (int u = 0; u < PRE; ++u) proc<false, false>(P[u], q, m, l, acc, rk, rv, vo, so);
#pragma unroll 1
    for (int it = PRE + 2; it < nit; ++it) {
      so += it <= NIT - DLT ? 32768 : 16384;
      proc<true, false>(A, q, m, l, acc, rk, rv, vo, so);
    }
    proc<false, true>(A, q, m, l, acc, rk, rv, vo, so);
    const v2f o2 = tr_reduce(acc);
    *(LAS v2f*)(sm + L_PART + sw * 512 + lane * 8) = o2;
    if (lane == 0) *(LAS v2f*)(sm + L_ML + sw * 8) = v2f{m, l};
    STAMP(2);
    bar();
    tag = __builtin_amdgcn_readfirstlane(tag);
    if (side == 0) {
      if (sw != 0) return;
      const int rb = pr >> 2, cg = (pr & 3) * 8 + xc, p8 = lane >> 3, c8 = lane & 7;
      const unsigned xv = *(const unsigned*)(x + rb * D + cg * 16 + c8 * 2);
      const int zo = WS_Z + (rb * 32 + cg) * 8192, zv = p8 * 1024 + c8 * 16;
      const int sv = WS_S + ((cg >> 3) * 4 + rb) * 512 + p8 * 64 + (c8 & 3) * 16;
      v4u z[8], sg;
      unsigned pend = 0x1ff;
#pragma unroll 1
      for (int spin = 0; spin < SPIN_CAP; ++spin) {
        asm volatile("" ::: "memory");
#pragma unroll
        for (int j = 0; j < 8; ++j) if (pend & (1u << j)) z[j] = LD128(rws, zv + j * 128, zo, 16);
        if (pend & 0x100) sg = LD128(rws, sv, 0, 16);
#pragma unroll
        for (int j = 0; j < 8; ++j) if ((pend & (1u << j)) && __all(z[j][1] == tag && z[j][3] == tag)) pend &= ~(1u << j);
        if ((pend & 0x100) && __all(sg[1] == tag && sg[3] == tag)) pend &= ~0x100u;
        if (!pend) break;
        __builtin_amdgcn_s_sleep(SLEEPN);
      }
      STAMP(3);
      const float dl = asf(sg[2]) - asf(sg[0]);
      const float w0 = __builtin_amdgcn_rcpf(1.f + __builtin_amdgcn_exp2f(dl)), w1 = __builtin_amdgcn_rcpf(1.f + __builtin_amdgcn_exp2f(-dl));
      float a0 = 0.f, a1 = 0.f;
#define ZACC(J, CT) { const float u0 = dpp<CT>(w0), u1 = dpp<CT>(w1); \
        a0 = fmaf(u0, asf(z[2 * J][0]), a0); a1 = fmaf(u0, asf(z[2 * J][2]), a1); \
        a0 = fmaf(u1, asf(z[2 * J + 1][0]), a0); a1 = fmaf(u1, asf(z[2 * J + 1][2]), a1); }
      ZACC(0, 0x00) ZACC(1, 0x55) ZACC(2, 0xAA) ZACC(3, 0xFF)
#undef ZACC
      a0 += dpp<ROR8>(a0); a1 += dpp<ROR8>(a1);
      a0 = xrow_sum(a0); a1 = xrow_sum(a1);
      const unsigned at = pk(a0, a1);
      const unsigned hp = pk(blo(xv) + blo(at), bhi(xv) + bhi(at));
      const unsigned hn = dppu<XOR1>(hp);
      const v4u g = (c8 & 1) ? v4u{hn, tag, hp, tag} : v4u{hp, tag, hn, tag};
      const int rep = p8 * 2 + (c8 & 1);
      if (rep < HREP) __builtin_amdgcn_raw_buffer_store_b128(g, rws, WS_H + rep * 8192 + rb * 2048 + cg * 64 + (c8 >> 1) * 16, 0, 16);
      STAMP(4);
      return;
    }
  } else {
    const int hw = w;
    unsigned t6 = 0, t7 = 0;
    if (hw == 0) {
      const __amdgpu_buffer_rsrc_t rq = RSRC(wq, D * 4096 * 2);
      const u16* xr = x + (c >> 2) * D;
      v4u xs[4], xa[2], W[16];
#pragma unroll
      for (int i = 0; i < 4; ++i) xs[i] = *(const v4u*)(xr + (r * 4 + (c & 3)) * 32 + i * 8);
#pragma unroll
      for (int i = 0; i < 2; ++i) xa[i] = *(const v4u*)(xr + jj * 64 + i * 32 + r * 8);
      const unsigned ev = __builtin_amdgcn_raw_buffer_load_b32(rws, 0, 0, 16);
      const int vo = r * (8 * 8192) + c * 16, so = jj * (64 * 8192) + h * 256;
#pragma unroll
      for (int i = 0; i < 16; ++i) W[i] = LD128(rq, vo, so + ((i >> 3) * 32 + (i & 7)) * 8192, WAUX);
      SB();
      float s0 = 0.f, s1 = 0.f;
#pragma unroll
      for (int i = 0; i < 4; ++i) { s0 = dot2(xs[i][0], 0x3f803f80u, s0); s1 = dot2(xs[i][1], 0x3f803f80u, s1); s0 = dot2(xs[i][2], 0x3f803f80u, s0); s1 = dot2(xs[i][3], 0x3f803f80u, s1); }
      float sx = s0 + s1; sx += dpp<XOR1>(sx); sx += dpp<XOR2>(sx); sx = xrow_sum(sx);
      const float mean = sx * (1.f / 512.f);
      float v0 = 0.f, v1 = 0.f;
#pragma unroll
      for (int i = 0; i < 4; ++i)
#pragma unroll
        for (int k = 0; k < 4; ++k) { const float d0 = blo(xs[i][k]) - mean, d1 = bhi(xs[i][k]) - mean; v0 = fmaf(d0, d0, v0); v1 = fmaf(d1, d1, v1); }
      float vs = v0 + v1; vs += dpp<XOR1>(vs); vs += dpp<XOR2>(vs); vs = xrow_sum(vs);
      const v2f st = ln_fin(mean, vs);
#pragma unroll
      for (int i = 0; i < 2; ++i)
#pragma unroll
        for (int k = 0; k < 4; ++k) xa[i][k] = ln_apply(xa[i][k], st);
      tag = __builtin_amdgcn_readfirstlane(ev) + 1u;
      t6 = TNOW();
      v4f qa[8];
#pragma unroll
      for (int f = 0; f < 8; ++f) qa[f] = v4f{0.f, 0.f, 0.f, 0.f};
#pragma unroll
      for (int i = 0; i < 2; ++i)
#pragma unroll
        for (int f = 0; f < 8; ++f) {
          const unsigned sel = (f & 1) ? 0x07060302u : 0x05040100u;
          v4u o;
#pragma unroll
          for (int k = 0; k < 4; ++k) o[k] = __builtin_amdgcn_perm(W[8 * i + 2 * k + 1][f >> 1], W[8 * i + 2 * k][f >> 1], sel);
          qa[f] = __builtin_amdgcn_mfma_f32_16x16x32_bf16(__builtin_bit_cast(bf16x8, xa[i]), __builtin_bit_cast(bf16x8, o), qa[f], 0, 0, 0);
        }
#pragma unroll
      for (int t = 0; t < 4; ++t)
        if (NOQ < 2 || qa[2 * t][0] == 1.2345f) __builtin_amdgcn_raw_buffer_store_b128(v4u{asu(qa[2 * t][0]), tag, asu(qa[2 * t + 1][0]), tag}, rws, r * 8192 + c * 16 + t * 256, WS_Q + h * 32768 + jj * 1024, 16);
      t7 = TNOW();
    } else if (hw == 1) {
      const unsigned ev = __builtin_amdgcn_raw_buffer_load_b32(rws, 0, 0, 16);
      tag = __builtin_amdgcn_readfirstlane(ev) + 1u;
      v4u g[8];
      unsigned pend = 0xff;
      const int qo = WS_Q + h * 32768 + b * 8192;
#pragma unroll 1
      for (int spin = 0; spin < SPIN_CAPQ; ++spin) {
        asm volatile("" ::: "memory");
#pragma unroll
        for (int j = 0; j < 8; ++j) if (pend & (1u << j)) g[j] = LD128(rws, lane * 16, qo + j * 1024, 16);
#pragma unroll
        for (int j = 0; j < 8; ++j) if ((pend & (1u << j)) && __all(g[j][1] == tag && g[j][3] == tag)) pend &= ~(1u << j);
        if (!pend) break;
        __builtin_amdgcn_s_sleep(SLEEPN);
      }
      const float q0 = ((asf(g[0][0]) + asf(g[1][0])) + (asf(g[2][0]) + asf(g[3][0]))) + ((asf(g[4][0]) + asf(g[5][0])) + (asf(g[6][0]) + asf(g[7][0])));
      const float q1 = ((asf(g[0][2]) + asf(g[1][2])) + (asf(g[2][2]) + asf(g[3][2]))) + ((asf(g[4][2]) + asf(g[5][2])) + (asf(g[6][2]) + asf(g[7][2])));
      *(LAS unsigned*)(sm + L_QP + (c * 4 + r) * 4) = pk(q0, q1);
      if (lane == 0) *(LAS unsigned*)(sm + L_TAG) = tag;
    }
    bar();
    STAMP(1); STAMPV(6, t6); STAMPV(7, t7);
    tag = __builtin_amdgcn_readfirstlane(*(LAS unsigned*)(sm + L_TAG));
    const int rot = ROTO * jj;
    unsigned WO[16][8];
    {
      const __amdgpu_buffer_rsrc_t ro = RSRC(wo, D * 4096 * 2);
      const int vo = r * 2048 + c * 16, so = h * 131072 + hw * 256;
#pragma unroll
      for (int g4 = 0; g4 < 16 / ORND; ++g4) {
        v4u t[2 * ORND];
#pragma unroll
        for (int u = 0; u < ORND; ++u) { const int su = so + ((g4 * ORND + u + rot) & 15) * 8192;
          t[2 * u] = NOWO ? v4u{tag, tag, tag, tag} : LD128(ro, vo, su, OAUX); t[2 * u + 1] = NOWO ? v4u{tag, tag, tag, tag} : LD128(ro, vo + 1024, su, OAUX); }
#pragma unroll
        for (int u = 0; u < ORND; ++u)
#pragma unroll
          for (int j = 0; j < 4; ++j) { WO[g4 * ORND + u][2 * j] = __builtin_amdgcn_perm(t[2 * u + 1][j], t[2 * u][j], 0x05040100);
            WO[g4 * ORND + u][2 * j + 1] = __builtin_amdgcn_perm(t[2 * u + 1][j], t[2 * u][j], 0x07060302); }
#pragma unroll
        for (int u = 0; u < ORND; ++u)
#pragma unroll
          for (int j = 0; j < 8; ++j) asm volatile("" : "+v"(WO[g4 * ORND + u][j]));
        if (OSLEEP) __builtin_amdgcn_s_sleep(OSLEEP);
        SB();
      }
    }
    if (FMODE) {
      if (isGU) {
#pragma unroll 1
        for (int ww = hw; ww < 8; ww += 4) stage_gu<FMODE>(sm, wg, wu, fg, ww, lane);
      } else if (isDN) {
#pragma unroll 1
        for (int ww = hw; ww < 7; ww += 4) stage_dn<FMODE>(sm, wd, cgD, ww, lane);
      }
    }
    const int pl = (r * 16 + ((c + rot) & 15)) * 8;
    STAMP(2);
    bar();
    {
      const v4f ml0 = *(LAS v4f*)(sm + L_ML), ml1 = *(LAS v4f*)(sm + L_ML + 16);
      const v2f p0 = *(LAS v2f*)(sm + L_PART + pl), p1 = *(LAS v2f*)(sm + L_PART + 512 + pl);
      const v2f p2 = *(LAS v2f*)(sm + L_PART + 1024 + pl), p3 = *(LAS v2f*)(sm + L_PART + 1536 + pl);
      const float M = fmaxf(fmaxf(ml0[0], ml0[2]), fmaxf(ml1[0], ml1[2]));
      const float e0 = __builtin_amdgcn_exp2f((ml0[0] - M) * L2E), e1 = __builtin_amdgcn_exp2f((ml0[2] - M) * L2E);
      const float e2 = __builtin_amdgcn_exp2f((ml1[0] - M) * L2E), e3 = __builtin_amdgcn_exp2f((ml1[2] - M) * L2E);
      const float L = (e0 * ml0[1] + e1 * ml0[3]) + (e2 * ml1[1] + e3 * ml1[3]);
      const float rl = __builtin_amdgcn_rcpf(L);
      const unsigned op = pk(((e0 * p0[0] + e1 * p1[0]) + (e2 * p2[0] + e3 * p3[0])) * rl, ((e0 * p0[1] + e1 * p1[1]) + (e2 * p2[1] + e3 * p3[1])) * rl);
      const int p = h * 2 + side;
      if (lane == 0) __builtin_amdgcn_raw_buffer_store_b64(v2u{asu(fmaf(M, L2E, __builtin_amdgcn_logf(L))), tag}, rws, WS_S + (hw * 4 + b) * 512 + p * 8, 0, 16);
      float za[8] = {0.f, 0.f, 0.f, 0.f, 0.f, 0.f, 0.f, 0.f};
#define ZSTEP(U) { const unsigned ob = dppu<BCAST + U>(op); _Pragma("unroll") for (int j = 0; j < 8; ++j) za[j] = dot2(WO[U][j], ob, za[j]); }
      ZSTEP(0) ZSTEP(1) ZSTEP(2) ZSTEP(3) ZSTEP(4) ZSTEP(5) ZSTEP(6) ZSTEP(7)
      ZSTEP(8) ZSTEP(9) ZSTEP(10) ZSTEP(11) ZSTEP(12) ZSTEP(13) ZSTEP(14) ZSTEP(15)
#undef ZSTEP
      const v2f z2 = tr_reduce(za);
      if (NOZ < 2 || z2[0] == 1.2345f) __builtin_amdgcn_raw_buffer_store_b128(v4u{asu(z2[0]), tag, asu(z2[1]), tag}, rws,
          (c >> 1) * 8192 + (c & 1) * 64 + r * 16, WS_Z + (b * 32 + hw * 8) * 8192 + p * 128, 16);
    }
    STAMP(3);
  }

  if (isGU) {
    if (!FMODE) { stage_gu<0>(sm, wg, wu, fg, w, lane); bar(); }
    STAMP(7);
    v4u fr[16];
#pragma unroll
    for (int s = 0; s < 16; ++s) fr[s] = *(LAS v4u*)(sm + L_FR + w * FRT + (s * 64 + lane) * 16);
    if (w >= 4) {
      const int hv = WS_H + (fg % HREP) * 8192 + (w - 4) * 2048 + lane * 32;
      v4u g0, g1;
#pragma unroll 1
      for (int spin = 0; spin < SPIN_CAP; ++spin) {
        asm volatile("" ::: "memory");
        g0 = LD128(rws, hv, 0, 16); g1 = LD128(rws, hv + 16, 0, 16);
        if (__all(g0[1] == tag && g0[3] == tag && g1[1] == tag && g1[3] == tag)) break;
        __builtin_amdgcn_s_sleep(SLEEPN);
      }
      STAMP(4);
      const v2f st = ln_stats(v4u{g0[0], g0[2], g1[0], g1[2]});
      LAS char* hd = sm + L_HN + (w - 4) * 1088 + (lane >> 3) * 128 + (lane & 1) * 64 + ((lane >> 1) & 3) * 4;
      *(LAS unsigned*)(hd) = ln_apply(g0[0], st); *(LAS unsigned*)(hd + 16) = ln_apply(g0[2], st);
      *(LAS unsigned*)(hd + 32) = ln_apply(g1[0], st); *(LAS unsigned*)(hd + 48) = ln_apply(g1[2], st);
    }
    bar();
    v4f acc = {0.f, 0.f, 0.f, 0.f};
#pragma unroll
    for (int s = 0; s < 16; ++s) {
      const v4u hb = *(LAS v4u*)(sm + L_HN + (c & 3) * 1088 + s * 64 + r * 16);
      acc = __builtin_amdgcn_mfma_f32_16x16x32_bf16(__builtin_bit_cast(bf16x8, fr[s]), __builtin_bit_cast(bf16x8, hb), acc, 0, 0, 0);
    }
    const unsigned gg = pk(acc[0], acc[2]), uu = pk(acc[1], acc[3]);
    const unsigned sg = pk(__builtin_amdgcn_rcpf(1.f + __builtin_amdgcn_exp2f(-L2E * blo(gg))), __builtin_amdgcn_rcpf(1.f + __builtin_amdgcn_exp2f(-L2E * bhi(gg))));
    const unsigned hh = pk(blo(sg) * blo(uu), bhi(sg) * bhi(uu));
    const int o = fg * 8 + w, line = (o >> 4) * 16 + (o & 3) * 4 + ((o >> 2) & 3);
    __builtin_amdgcn_raw_buffer_store_b64(v2u{hh, tag}, rws, (c >> 2) * 32768 + (c & 3) * 32 + r * 8, WS_HH + line * 128, 16);
    STAMP(5);
  } else if (isDN) {
    float res = 0.f;
    if (w < 7) {
      if (!FMODE) { stage_dn<0>(sm, wd, cgD, w, lane); asm volatile("s_waitcnt lgkmcnt(0)" ::: "memory"); }
      STAMP(7);
      v4u fr[8];
#pragma unroll
      for (int s = 0; s < 8; ++s) fr[s] = *(LAS v4u*)(sm + L_FR + w * 8704 + s * 1088 + r * 272 + c * 16);
      const int hv = WS_HH + (cgD & 3) * 32768 + w * 4096 + lane * 32;
      v4u g[4];
#pragma unroll 1
      for (int spin = 0; spin < SPIN_CAP; ++spin) {
        asm volatile("" ::: "memory");
        g[0] = LD128(rws, hv, 0, 16); g[1] = LD128(rws, hv + 16, 0, 16); g[2] = LD128(rws, hv + 2048, 0, 16); g[3] = LD128(rws, hv + 2064, 0, 16);
        if (__all(g[0][1] == tag && g[0][3] == tag && g[1][1] == tag && g[1][3] == tag && g[2][1] == tag && g[2][3] == tag && g[3][1] == tag && g[3][3] == tag)) break;
        __builtin_amdgcn_s_sleep(SLEEPN);
      }
      STAMP(4);
      const bf16x8 b0 = __builtin_bit_cast(bf16x8, v4u{g[0][0], g[0][2], g[1][0], g[1][2]});
      const bf16x8 b1 = __builtin_bit_cast(bf16x8, v4u{g[2][0], g[2][2], g[3][0], g[3][2]});
      v4f ac[4];
#pragma unroll
      for (int i = 0; i < 4; ++i) ac[i] = __builtin_amdgcn_mfma_f32_16x16x32_bf16(__builtin_bit_cast(bf16x8, fr[i]), b0, v4f{0.f, 0.f, 0.f, 0.f}, 0, 0, 0);
#pragma unroll
      for (int i = 0; i < 4; ++i) ac[i] = __builtin_amdgcn_mfma_f32_16x16x32_bf16(__builtin_bit_cast(bf16x8, fr[4 + i]), b1, ac[i], 0, 0, 0);
      v4f sum;
#pragma unroll
      for (int k = 0; k < 4; ++k) sum[k] = (ac[0][k] + dpp<SHL + 4>(ac[1][k])) + (dpp<SHL + 8>(ac[2][k]) + dpp<SHL + 12>(ac[3][k]));
      if (c < 4) *(LAS v4f*)(sm + L_RED + w * 256 + (r * 4 + c) * 16) = sum;
    } else {
      const int hv = WS_H + ((28 + cgD) % HREP) * 8192 + (c & 3) * 2048 + (cgD * 8 + r * 2 + (c >> 3)) * 8;
      v2u g;
#pragma unroll 1
      for (int spin = 0; spin < SPIN_CAP; ++spin) {
        asm volatile("" ::: "memory");
        g = __builtin_amdgcn_raw_buffer_load_b64(rws, hv, 0, 16);
        if (__all(g[1] == tag)) break;
        __builtin_amdgcn_s_sleep(SLEEPN);
      }
      res = (c & 4) ? bhi(g[0]) : blo(g[0]);
    }
    bar();
    if (w == 7) {
      float s = 0.f;
#pragma unroll
      for (int i = 0; i < 7; ++i) s += *(LAS float*)(sm + L_RED + i * 256 + (r * 4 + (c & 3)) * 16 + (c >> 2) * 4);
      const unsigned o = pk(res + rbf(s), 0.f);
      out[(c & 3) * D + cgD * 16 + r * 4 + (c >> 2)] = (u16)o;
      if (cgD == 0 && lane == 0) __builtin_amdgcn_raw_buffer_store_b32(tag, rws, 0, 0, 16);
      STAMP(5);
    }
  }
}

extern "C" void kernel_launch(void* const* d_in, const int* in_sizes, int n_in, void* d_out, int out_size, void* d_ws, size_t ws_size, hipStream_t stream) {
  fused_block<<<256, 512, 0, stream>>>((const u16*)d_in[0], (const u16*)d_in[1], (const u16*)d_in[2], (const u16*)d_in[3], (const u16*)d_in[4],
                                       (const u16*)d_in[6], (const u16*)d_in[7], (const u16*)d_in[8], (u16*)d_out, (char*)d_ws);
}
```

```cpp
#include <hip/hip_runtime.h>
#include <hip/hip_bf16.h>
#include <cstdint>

typedef unsigned short u16;
typedef unsigned v4u __attribute__((ext_vector_type(4)));
typedef unsigned v2u __attribute__((ext_vector_type(2)));
typedef float v4f __attribute__((ext_vector_type(4)));
typedef float v2f __attribute__((ext_vector_type(2)));
typedef __bf16 bf16x2 __attribute__((ext_vector_type(2)));
typedef __bf16 bf16x8 __attribute__((ext_vector_type(8)));
#define LAS __attribute__((address_space(3)))

#define PRE 3
#define PACE 8
#define HREP 16
#define GREP 4
#define GAUX 2
#define DAUX 2
#define BNS 2
#define CNS 1
#define STG 7
#define ADAPT 1
#define T1 2400
#define T2 3600
#define MARG 210
#define EPS 1.0f
#define RLG 200
#define RLD 400
#define GA 170
#define GB 0
#define GC 0
#define KROT 0
#define ILV 0

constexpr int D = 512, F = 1792, NG = 56;
constexpr int NIT = 64;
constexpr int WS_H = 65536;
constexpr int WS_HH = 262144;
constexpr int WS_ML = 524288;
constexpr int WS_PG = 655360;
constexpr int WS_TE = 720896;
constexpr int WS_Z = 1048576;
constexpr int WS_Q = 2097152;
constexpr int WS_BYTES = 3145728;
constexpr int SPIN_CAP = 400000;
constexpr int L_FR = 0;
constexpr int FRT = 16384 + 16;
constexpr int L_SC = 4 * FRT;
constexpr int L_QP = L_SC + 16384;
constexpr int L_PART = L_QP + 256;
constexpr int L_ML = L_PART + 2048;
constexpr int L_FA = L_ML + 32;
constexpr int L_TAG = L_FA + 16;
constexpr int L_TE = L_TAG + 4, L_PA = L_TAG + 8, L_PB = L_TAG + 12, L_CNT = L_TAG + 16;
constexpr int L_HN = L_TAG + 32;
constexpr int L_RED = L_HN + 4352;
constexpr int L_END = L_RED + 1792;

constexpr float L2E = 1.4426950408889634f;

__device__ __forceinline__ float asf(unsigned u) { return __builtin_bit_cast(float, u); }
__device__ __forceinline__ unsigned asu(float f) { return __builtin_bit_cast(unsigned, f); }
__device__ __forceinline__ float blo(unsigned u) { return asf(u << 16); }
__device__ __forceinline__ float bhi(unsigned u) { return asf(u & 0xffff0000u); }
__device__ __forceinline__ unsigned pk(float a, float b) {
  bf16x2 r; r[0] = (__bf16)a; r[1] = (__bf16)b; return __builtin_bit_cast(unsigned, r);
}
__device__ __forceinline__ float rbf(float f) { __bf16 h = (__bf16)f; return (float)h; }
__device__ __forceinline__ float dot2(unsigned a, unsigned b, float acc) {
  return __builtin_amdgcn_fdot2_f32_bf16(__builtin_bit_cast(bf16x2, a), __builtin_bit_cast(bf16x2, b), acc, false);
}
template <int CTRL> __device__ __forceinline__ unsigned dppu(unsigned x) {
  return (unsigned)__builtin_amdgcn_mov_dpp((int)x, CTRL, 0xf, 0xf, true);
}
template <int CTRL> __device__ __forceinline__ float dpp(float x) { return asf(dppu<CTRL>(asu(x))); }
constexpr int XOR1 = 0xB1, XOR2 = 0x4E, HMIR = 0x141, ROR8 = 0x128, BCAST = 0x150, SHL = 0x100;
__device__ __forceinline__ float row_sum(float x) {
  x += dpp<XOR1>(x); x += dpp<XOR2>(x); x += dpp<HMIR>(x); x += dpp<ROR8>(x); return x;
}
__device__ __forceinline__ float xrow_sum(float x) {
  auto s = __builtin_amdgcn_permlane16_swap(asu(x), asu(x), false, false); x = asf(s[0]) + asf(s[1]);
  auto t = __builtin_amdgcn_permlane32_swap(asu(x), asu(x), false, false); return asf(t[0]) + asf(t[1]);
}
__device__ __forceinline__ float xrow_max(float x) {
  auto s = __builtin_amdgcn_permlane16_swap(asu(x), asu(x), false, false); x = fmaxf(asf(s[0]), asf(s[1]));
  auto t = __builtin_amdgcn_permlane32_swap(asu(x), asu(x), false, false); return fmaxf(asf(t[0]), asf(t[1]));
}
__device__ __forceinline__ v2f tr_reduce(const float (&a)[8]) {
  float b[4];
#pragma unroll
  for (int j = 0; j < 4; ++j) { auto s = __builtin_amdgcn_permlane32_swap(asu(a[j]), asu(a[j + 4]), false, false); b[j] = asf(s[0]) + asf(s[1]); }
  v2f c;
#pragma unroll
  for (int j = 0; j < 2; ++j) { auto s = __builtin_amdgcn_permlane16_swap(asu(b[j]), asu(b[j + 2]), false, false); c[j] = asf(s[0]) + asf(s[1]); }
  return c;
}
__device__ __forceinline__ v2f ln_fin(float mean, float var) {
  const float sd = rbf(__builtin_amdgcn_sqrtf(rbf(rbf(var) + 1e-5f)));
  return v2f{rbf(mean), __builtin_amdgcn_rcpf(sd)};
}
__device__ __forceinline__ v2f ln_stats(v4u in) {
  const float c0 = blo(__builtin_amdgcn_readfirstlane(in[0]));
  float s0 = 0.f, s1 = 0.f, q0 = 0.f, q1 = 0.f;
#pragma unroll
  for (int j = 0; j < 4; ++j) { const float d0 = blo(in[j]) - c0, d1 = bhi(in[j]) - c0; s0 += d0; s1 += d1; q0 = fmaf(d0, d0, q0); q1 = fmaf(d1, d1, q1); }
  auto u = __builtin_amdgcn_permlane16_swap(asu(s0 + s1), asu(q0 + q1), false, false);
  float t = asf(u[0]) + asf(u[1]);
  auto v = __builtin_amdgcn_permlane32_swap(asu(t), asu(t), false, false);
  t = row_sum(asf(v[0]) + asf(v[1]));
  const float md = asf(__builtin_amdgcn_readlane(asu(t), 0)) * (1.f / 512.f), sq = asf(__builtin_amdgcn_readlane(asu(t), 16)) * (1.f / 512.f);
  return ln_fin(c0 + md, fmaxf(fmaf(-md, md, sq), 0.f));
}
__device__ __forceinline__ unsigned ln_apply(unsigned in, v2f st) {
  const unsigned d = pk(blo(in) - st[0], bhi(in) - st[0]); return pk(blo(d) * st[1], bhi(d) * st[1]);
}
__device__ __forceinline__ void bar() { asm volatile("s_waitcnt lgkmcnt(0)\n\ts_barrier" ::: "memory"); }
#define SB() __builtin_amdgcn_sched_barrier(0)
#define RSRC(p, n) __builtin_amdgcn_make_buffer_rsrc((void*)(p), 0, (int)(n), 0x00020000)
#define LD128(r, vo, so, aux) __builtin_amdgcn_raw_buffer_load_b128(r, vo, so, aux)
#define MFMA(a, b, c) __builtin_amdgcn_mfma_f32_16x16x32_bf16(__builtin_bit_cast(bf16x8, a), __builtin_bit_cast(bf16x8, b), c, 0, 0, 0)
#define LDSADD(off, v) __hip_atomic_fetch_add((LAS unsigned*)(sm + (off)), (unsigned)(v), __ATOMIC_RELAXED, __HIP_MEMORY_SCOPE_WORKGROUP)
#define LDSV(off) (*(volatile LAS unsigned*)(sm + (off)))
__device__ __forceinline__ unsigned clk() { return (unsigned)__builtin_amdgcn_s_memrealtime(); }
template <int N> __device__ __forceinline__ void until(unsigned t) {
#pragma unroll 1
  for (int k = 0; k < 4000; ++k) { if ((int)(clk() - t) >= 0) break; __builtin_amdgcn_s_sleep(N); }
}
#define GATE(G) do { if (G) { const unsigned te_ = __builtin_amdgcn_readfirstlane(LDSV(L_TE)); if (te_) until<1>(te_ + (G)); } } while (0)

#define STAMP(i) do {} while (0)
#define NOTE(i, v) do {} while (0)
#define FLUSH(role) do {} while (0)

#define ALD(dst, off) asm volatile("buffer_load_dwordx4 %0, %1, %2, 0 offen offset:%3 sc1" : "=v"(dst) : "v"(hv), "s"(rs), "n"(off) : "memory")
template <int NL, int NS>
__device__ __forceinline__ int ppoll(const v4u rs, int hv, unsigned tag, v4u (&g)[NS][NL], unsigned (&vals)[2 * NL]) {
  asm volatile("s_waitcnt vmcnt(0)" ::: "memory");
#pragma unroll
  for (int s = 0; s < NS; ++s) {
#pragma unroll
    for (int i = 0; i < NL; ++i) ALD(g[s][i], i * 1024);
    if (s < NS - 1) __builtin_amdgcn_s_sleep(STG);
  }
  int spin = 0;
#pragma unroll 1
  for (; spin < SPIN_CAP; ++spin) {
#pragma unroll
    for (int s = 0; s < NS; ++s) {
      if (NL == 2) asm volatile("s_waitcnt vmcnt(%2)" : "+v"(g[s][0]), "+v"(g[s][1]) : "n"((NS - 1) * NL) : "memory");
      else asm volatile("s_waitcnt vmcnt(%4)" : "+v"(g[s][0]), "+v"(g[s][1]), "+v"(g[s][NL - 2]), "+v"(g[s][NL - 1]) : "n"((NS - 1) * NL) : "memory");
      unsigned bad = 0;
#pragma unroll
      for (int i = 0; i < NL; ++i) { bad |= (g[s][i][1] ^ tag) | (g[s][i][3] ^ tag); vals[2 * i] = g[s][i][0]; vals[2 * i + 1] = g[s][i][2]; }
#pragma unroll
      for (int i = 0; i < 2 * NL; ++i) asm volatile("" : "+v"(vals[i]));
      if (__all(bad == 0)) return spin * NS + s;
#pragma unroll
      for (int i = 0; i < NL; ++i) ALD(g[s][i], i * 1024);
    }
  }
  return spin;
}
template <int NL, int NS> __device__ __forceinline__ void keep(v4u (&g)[NS][NL]) {
  asm volatile("s_waitcnt vmcnt(0)" ::: "memory");
#pragma unroll
  for (int s = 0; s < NS; ++s)
#pragma unroll
    for (int i = 0; i < NL; ++i) asm volatile("" :: "v"(g[s][i]));
}

constexpr int LSTEP = ILV == 2 ? 32768 : 4096;
template <bool RE>
__device__ __forceinline__ void prock(v4u (&a)[8], const v4u q, float& mx, LAS char* dst, bool lo8, bool q1,
                                      const __amdgpu_buffer_rsrc_t rn, int vo, int so) {
  float x[8];
#pragma unroll
  for (int i = 0; i < 8; ++i) {
    x[i] = dot2(a[i][3], q[3], dot2(a[i][2], q[2], dot2(a[i][1], q[1], dot2(a[i][0], q[0], 0.f))));
    asm volatile("" : "+v"(x[i]));
    SB();
    if (RE) a[i] = LD128(rn, vo + (i & 3) * 1024, so + (i >> 2) * LSTEP, 2);
    SB();
  }
  float y[4], z[2];
#pragma unroll
  for (int j = 0; j < 4; ++j) { const float u = x[j] + dpp<ROR8>(x[j]), v = x[j + 4] + dpp<ROR8>(x[j + 4]); y[j] = lo8 ? u : v; }
#pragma unroll
  for (int j = 0; j < 2; ++j) { const float u = y[j] + dpp<HMIR>(y[j]), v = y[j + 2] + dpp<HMIR>(y[j + 2]); z[j] = q1 ? v : u; z[j] += dpp<XOR1>(z[j]); z[j] += dpp<XOR2>(z[j]); }
  const unsigned r01 = pk(z[0], z[1]);
  const float SC = 0.08838834764831845f;
  const unsigned t01 = pk(blo(r01) * SC, bhi(r01) * SC);
  const v2f s = {blo(t01), bhi(t01)};
  mx = fmaxf(fmaxf(s[0], s[1]), mx);
  *(LAS v2f*)dst = s;
  SB();
}
template <bool RE>
__device__ __forceinline__ void procv(v4u (&a)[8], const LAS char* src, float (&acc)[8], const __amdgpu_buffer_rsrc_t rn, int vo, int so) {
  const v4f p0 = *(const LAS v4f*)src, p1 = *(const LAS v4f*)(src + 16);
  SB();
#pragma unroll
  for (int i = 0; i < 8; ++i) {
    const float p = i < 4 ? p0[i & 3] : p1[i & 3];
#pragma unroll
    for (int k = 0; k < 4; ++k) { acc[2 * k] = fmaf(p, blo(a[i][k]), acc[2 * k]); acc[2 * k + 1] = fmaf(p, bhi(a[i][k]), acc[2 * k + 1]); }
    asm volatile("" : "+v"(acc[0]), "+v"(acc[1]), "+v"(acc[2]), "+v"(acc[3]), "+v"(acc[4]), "+v"(acc[5]), "+v"(acc[6]), "+v"(acc[7]));
    SB();
    if (RE) a[i] = LD128(rn, vo + (i & 3) * 1024, so + (i >> 2) * LSTEP, 2);
    SB();
  }
}

__device__ __forceinline__ v4u tcol(const v4u* t, int f) {
  const unsigned sel = (f & 1) ? 0x07060302u : 0x05040100u;
  v4u o;
#pragma unroll
  for (int j = 0; j < 4; ++j) o[j] = __builtin_amdgcn_perm(t[2 * j + 1][f >> 1], t[2 * j][f >> 1], sel);
  return o;
}

__global__ void __launch_bounds__(512, 2)
fused_block(const u16* __restrict__ x, const u16* __restrict__ kc, const u16* __restrict__ vc, const u16* __restrict__ wq,
            const u16* __restrict__ wo, const u16* __restrict__ wg, const u16* __restrict__ wu, const u16* __restrict__ wd,
            u16* out, char* ws) {
  __shared__ __attribute__((aligned(16))) char smem_[L_END > 81920 + 16 ? L_END : 81920 + 16];
  LAS char* const sm = (LAS char*)smem_;
  const int tid = threadIdx.x, lane = tid & 63, w = __builtin_amdgcn_readfirstlane(tid >> 6);
  const int r = lane >> 4, c = lane & 15;
  const int bid = blockIdx.x, xg = (bid >> 1) & 3, slot = bid >> 3;
  const int xc = bid & 7, side = slot & 1, pr = slot >> 1;
  const int b = pr >> 2, bh = pr * 8 + xc, h = bh & 31;
  const bool isRED = bid & 1, isGU = !isRED && slot < NG / 4, isDN = !isRED && slot >= NG / 4 && slot < NG / 4 + 8;
  const int fg = xg * (NG / 4) + slot, cgD = xg * 8 + slot - NG / 4;
  const int jj = b * 2 + side;
  unsigned tag;
  STAMP(0);
  const unsigned t0 = clk();
  asm volatile("" :: "s"(x), "s"(kc), "s"(vc), "s"(wq), "s"(wo), "s"(ws));
  const __amdgpu_buffer_rsrc_t rws = RSRC(ws, WS_BYTES);
  const v4u rsw = {(unsigned)(uintptr_t)ws, (unsigned)((uintptr_t)ws >> 32), (unsigned)WS_BYTES, 0x00020000u};

  if (w >= 4) {
    const int sw = w - 4;
    const __amdgpu_buffer_rsrc_t rk = RSRC(kc, 0x10000000), rv = RSRC(vc, 0x10000000);
    const int vo = lane * 16, rot = bh * KROT;
    const int sb = bh * (2 << 20) + (ILV == 0 ? (side * 4 + sw) * 8192 : ILV == 1 ? side * (1 << 20) + sw * 8192 : (side * 4 + sw) * 4096);
#define SO(i) (sb + (((i) + rot) & 31) * (ILV == 1 ? 32768 : 65536))
    v4u P[PRE + 1][8];
#pragma unroll
    for (int u = 0; u <= PRE; ++u) {
#pragma unroll
      for (int i = 0; i < 8; ++i) {
        if (PACE && u * 8 + i >= PACE) { SB(); asm volatile("s_waitcnt vmcnt(%0)" :: "n"(PACE - 1)); SB(); }
        P[u][i] = LD128(rk, vo + (i & 3) * 1024, SO(u) + (i >> 2) * LSTEP, 2);
      }
    }
    SB();
    STAMP(6);
    bar();
    STAMP(1);
    const v4u q = *(LAS v4u*)(sm + L_QP + c * 16);
    tag = *(LAS unsigned*)(sm + L_TAG);
    LAS char* const sc = sm + L_SC + sw * 4096;
    LAS char* const sd = sc + r * 32 + (c >> 2) * 8;
    const bool lo8 = c < 8, q1 = c & 4;
    float mx = -3.0e38f;
    prock<true>(P[0], q, mx, sd, lo8, q1, rk, vo, SO(PRE + 1));
#pragma unroll
    for (int u = 1; u <= PRE; ++u) prock<false>(P[u], q, mx, sd + u * 128, lo8, q1, rk, vo, 0);
    int tau = 0, due = 0, ph = 0, it1 = 0, t1 = 0;
    unsigned tev = 0;
    auto pace = [&](int it) __attribute__((always_inline)) {
      if (!ADAPT) return;
      int now = (int)(clk() - t0);
      if (ph == 0) { if (now >= T1) { it1 = it; t1 = now; ph = 1; } }
      else if (ph == 1) {
        if (now >= T2) {
          if (lane == 0) { LDSADD(L_PB, (now - t1) | (min(now - T2, 4000) << 16)); LDSADD(L_PA, (it - it1) | (it << 10) | (1 << 28)); }
          ph = 2;
        }
      } else if (ph == 2) {
        const unsigned ta = __builtin_amdgcn_readfirstlane(tev);
        tev = LDSV(L_TE);
        if (ta) {
          const int te = (int)(ta - t0) - (isGU ? RLG : isDN ? RLD : 0);
          tau = min(max(__builtin_amdgcn_readfirstlane((int)((float)((te - now) * 64) * __builtin_amdgcn_rcpf((float)(NIT - it)))), 1), 64 * 250);
          due = now * 64; ph = 3;
        }
      }
      if (ph == 3) {
#pragma unroll 1
        for (int k = 0; k < 200; ++k) {
          if (now * 64 >= due) break;
          __builtin_amdgcn_s_sleep(1);
          now = (int)(clk() - t0);
        }
        due += tau;
      }
    };
#pragma unroll 1
    for (int it = PRE + 2; it <= NIT / 2; ++it) {
      pace(it);
      prock<true>(P[0], q, mx, sd + (it - 1) * 128, lo8, q1, RSRC(it < NIT / 2 ? kc : vc, 0x10000000), vo, SO(it));
    }
    {
      mx = fmaxf(mx, dpp<HMIR>(mx)); mx = fmaxf(mx, dpp<ROR8>(mx)); mx = xrow_max(mx);
      const float mL = mx * L2E;
      float l = 0.f;
#pragma unroll
      for (int j = 0; j < 4; ++j) {
        v4f s = *(LAS v4f*)(sc + j * 1024 + lane * 16);
#pragma unroll
        for (int k = 0; k < 4; ++k) s[k] = __builtin_amdgcn_exp2f(fmaf(s[k], L2E, -mL));
        l += (s[0] + s[1]) + (s[2] + s[3]);
        *(LAS v4f*)(sc + j * 1024 + lane * 16) = s;
      }
      l = xrow_sum(row_sum(l));
      if (lane == 0) { *(volatile LAS v2f*)(sm + L_ML + sw * 8) = v2f{mx, l}; LDSADD(L_CNT, 1); }
      asm volatile("" ::: "memory");
    }
    float acc[8] = {0.f, 0.f, 0.f, 0.f, 0.f, 0.f, 0.f, 0.f};
#pragma unroll 1
    for (int it = NIT / 2 + 1; it < NIT; ++it) {
      pace(it);
      procv<true>(P[0], sc + r * 32 + (it - NIT / 2 - 1) * 128, acc, rv, vo, SO(it));
    }
    procv<false>(P[0], sc + r * 32 + (NIT / 2 - 1) * 128, acc, rv, vo, 0);
    *(LAS v2f*)(sm + L_PART + sw * 512 + lane * 8) = tr_reduce(acc);
    STAMP(2);
    bar();
    tag = __builtin_amdgcn_readfirstlane(tag);
    if (isRED) {
      if (sw != 0) { FLUSH(1); return; }
      const int rb = xg, cg = slot, p8 = lane >> 3, c8 = lane & 7;
      const unsigned xv = *(const unsigned*)(x + rb * D + cg * 16 + c8 * 2);
      const int zo = WS_Z + (rb * 32 + cg) * 8192, zv = p8 * 1024 + c8 * 16;
      v4u z[8];
      GATE(GA);
      STAMP(7);
      unsigned pend = 0xff;
      int spin = 0;
#pragma unroll 1
      for (; spin < SPIN_CAP; ++spin) {
        asm volatile("" ::: "memory");
#pragma unroll
        for (int j = 0; j < 8; ++j) if (pend & (1u << j)) z[j] = LD128(rws, zv + j * 128, zo, 16);
#pragma unroll
        for (int j = 0; j < 8; ++j) if ((pend & (1u << j)) && __all(z[j][1] == tag && z[j][3] == tag)) pend &= ~(1u << j);
        if (!pend) break;
      }
      STAMP(4); NOTE(9, spin + 1);
      float a0 = ((asf(z[0][0]) + asf(z[1][0])) + (asf(z[2][0]) + asf(z[3][0]))) + ((asf(z[4][0]) + asf(z[5][0])) + (asf(z[6][0]) + asf(z[7][0])));
      float a1 = ((asf(z[0][2]) + asf(z[1][2])) + (asf(z[2][2]) + asf(z[3][2]))) + ((asf(z[4][2]) + asf(z[5][2])) + (asf(z[6][2]) + asf(z[7][2])));
      a0 += dpp<ROR8>(a0); a1 += dpp<ROR8>(a1);
      a0 = xrow_sum(a0); a1 = xrow_sum(a1);
      const unsigned at = pk(a0, a1);
      const unsigned hp = pk(blo(xv) + blo(at), bhi(xv) + bhi(at));
      const unsigned hn = dppu<XOR1>(hp);
      const v4u g = (c8 & 1) ? v4u{hn, tag, hp, tag} : v4u{hp, tag, hn, tag};
      const int rep = p8 * 2 + (c8 & 1);
      if (rep < HREP) __builtin_amdgcn_raw_buffer_store_b128(g, rws, WS_H + rep * 8192 + rb * 2048 + cg * 64 + (c8 >> 1) * 16, 0, 16);
      STAMP(5);
      FLUSH(1);
      return;
    }
  } else {
    const int hw = w;
    if (hw == 0) {
      const __amdgpu_buffer_rsrc_t rq = RSRC(wq, D * 4096 * 2);
      const u16* xr = x + (c >> 2) * D;
      v4u xs[4], xa[2], W[16];
#pragma unroll
      for (int i = 0; i < 4; ++i) xs[i] = *(const v4u*)(xr + (r * 4 + (c & 3)) * 32 + i * 8);
#pragma unroll
      for (int i = 0; i < 2; ++i) xa[i] = *(const v4u*)(xr + jj * 64 + i * 32 + r * 8);
      const unsigned ev = __builtin_amdgcn_raw_buffer_load_b32(rws, 0, 0, 16);
      const int vo = r * (8 * 8192) + c * 16, so = jj * (64 * 8192) + h * 256;
#pragma unroll
      for (int i = 0; i < 16; ++i) W[i] = LD128(rq, vo, so + ((i >> 3) * 32 + (i & 7)) * 8192, 2);
      SB();
      float s0 = 0.f, s1 = 0.f;
#pragma unroll
      for (int i = 0; i < 4; ++i) { s0 = dot2(xs[i][0], 0x3f803f80u, s0); s1 = dot2(xs[i][1], 0x3f803f80u, s1); s0 = dot2(xs[i][2], 0x3f803f80u, s0); s1 = dot2(xs[i][3], 0x3f803f80u, s1); }
      float sx = s0 + s1; sx += dpp<XOR1>(sx); sx += dpp<XOR2>(sx); sx = xrow_sum(sx);
      const float mean = sx * (1.f / 512.f);
      float v0 = 0.f, v1 = 0.f;
#pragma unroll
      for (int i = 0; i < 4; ++i)
#pragma unroll
        for (int k = 0; k < 4; ++k) { const float d0 = blo(xs[i][k]) - mean, d1 = bhi(xs[i][k]) - mean; v0 = fmaf(d0, d0, v0); v1 = fmaf(d1, d1, v1); }
      float vs = v0 + v1; vs += dpp<XOR1>(vs); vs += dpp<XOR2>(vs); vs = xrow_sum(vs);
      const v2f st = ln_fin(mean, vs * (1.f / 512.f));
#pragma unroll
      for (int i = 0; i < 2; ++i)
#pragma unroll
        for (int k = 0; k < 4; ++k) xa[i][k] = ln_apply(xa[i][k], st);
      tag = __builtin_amdgcn_readfirstlane(ev) + 1u;
      v4f qa[8];
#pragma unroll
      for (int f = 0; f < 8; ++f) qa[f] = MFMA(xa[0], tcol(W, f), (v4f{0.f, 0.f, 0.f, 0.f}));
#pragma unroll
      for (int f = 0; f < 8; ++f) qa[f] = MFMA(xa[1], tcol(W + 8, f), qa[f]);
#pragma unroll
      for (int t = 0; t < 4; ++t)
        __builtin_amdgcn_raw_buffer_store_b128(v4u{asu(qa[2 * t][0]), tag, asu(qa[2 * t + 1][0]), tag}, rws, r * 8192 + c * 16 + t * 256, WS_Q + h * 32768 + jj * 1024, 16);
      STAMP(6);
    } else if (hw == 1) {
      const unsigned ev = __builtin_amdgcn_raw_buffer_load_b32(rws, 0, 0, 16);
      tag = __builtin_amdgcn_readfirstlane(ev) + 1u;
      v4u g[8];
      unsigned pend = 0xff;
      const int qo = WS_Q + h * 32768 + b * 8192;
#pragma unroll 1
      for (int spin = 0; spin < SPIN_CAP; ++spin) {
        asm volatile("" ::: "memory");
#pragma unroll
        for (int j = 0; j < 8; ++j) if (pend & (1u << j)) g[j] = LD128(rws, lane * 16, qo + j * 1024, 16);
#pragma unroll
        for (int j = 0; j < 8; ++j) if ((pend & (1u << j)) && __all(g[j][1] == tag && g[j][3] == tag)) pend &= ~(1u << j);
        if (!pend) break;
      }
      const float q0 = ((asf(g[0][0]) + asf(g[1][0])) + (asf(g[2][0]) + asf(g[3][0]))) + ((asf(g[4][0]) + asf(g[5][0])) + (asf(g[6][0]) + asf(g[7][0])));
      const float q1 = ((asf(g[0][2]) + asf(g[1][2])) + (asf(g[2][2]) + asf(g[3][2]))) + ((asf(g[4][2]) + asf(g[5][2])) + (asf(g[6][2]) + asf(g[7][2])));
      *(LAS unsigned*)(sm + L_QP + (c * 4 + r) * 4) = pk(q0, q1);
      if (lane < 8) *(LAS unsigned*)(sm + L_TAG + lane * 4) = lane ? 0u : tag;
    }
    bar();
    STAMP(1);
    tag = __builtin_amdgcn_readfirstlane(*(LAS unsigned*)(sm + L_TAG));
    unsigned WO[16][8];
    {
      const __amdgpu_buffer_rsrc_t ro = RSRC(wo, D * 4096 * 2);
      const int vo = r * 2048 + c * 16, so = h * 131072 + hw * 256;
#pragma unroll
      for (int u = 0; u < 16; ++u) {
        const v4u t0 = LD128(ro, vo, so + u * 8192, 2), t1 = LD128(ro, vo + 1024, so + u * 8192, 2);
#pragma unroll
        for (int j = 0; j < 4; ++j) { WO[u][2 * j] = __builtin_amdgcn_perm(t1[j], t0[j], 0x05040100); WO[u][2 * j + 1] = __builtin_amdgcn_perm(t1[j], t0[j], 0x07060302); }
#pragma unroll
        for (int j = 0; j < 8; ++j) asm volatile("" : "+v"(WO[u][j]));
        SB();
      }
    }
    STAMP(2);
    if (hw == 2) {
#pragma unroll 1
      for (int spin = 0; spin < SPIN_CAP; ++spin) { if (__builtin_amdgcn_readfirstlane(LDSV(L_CNT)) == 4) break; __builtin_amdgcn_s_sleep(8); }
      asm volatile("" ::: "memory");
      const v2f ml = *(volatile LAS v2f*)(sm + L_ML + (lane & 3) * 8);
      float M = fmaxf(ml[0], dpp<XOR1>(ml[0])); M = fmaxf(M, dpp<XOR2>(M));
      float L = ml[1] * __builtin_amdgcn_exp2f((ml[0] - M) * L2E); L += dpp<XOR1>(L); L += dpp<XOR2>(L);
      if (lane < 8) __builtin_amdgcn_raw_buffer_store_b128(v4u{asu(M), tag, asu(L), tag}, rws, lane * 16, WS_ML + bid * 128, 16);
      v4u g;
#pragma unroll 1
      for (int spin = 0; spin < SPIN_CAP; ++spin) {
        __builtin_amdgcn_s_sleep(64);
        asm volatile("" ::: "memory");
        g = LD128(rws, 0, WS_ML + (bid ^ 8) * 128, 16);
        if (__all(g[1] == tag && g[3] == tag)) break;
      }
      const float Mo = asf(g[0]), Mt = fmaxf(M, Mo);
      const float La = L * __builtin_amdgcn_exp2f((M - Mt) * L2E), Lb = asf(g[2]) * __builtin_amdgcn_exp2f((Mo - Mt) * L2E);
      if (lane < 4) *(LAS float*)(sm + L_FA + lane * 4) = __builtin_amdgcn_exp2f((ml[0] - Mt) * L2E) / (La + Lb);
      STAMP(7);
    }
    if (ADAPT && hw == 3) {
      until<8>(t0 + T2);
      unsigned pa = 0;
#pragma unroll 1
      for (int k = 0; k < 100; ++k) { pa = __builtin_amdgcn_readfirstlane(LDSV(L_PA)); if ((pa >> 28) == 4) break; __builtin_amdgcn_s_sleep(2); }
      if ((pa >> 28) == 4) {
        const unsigned pb = LDSV(L_PB);
        if (lane < 8) __builtin_amdgcn_raw_buffer_store_b128(v4u{pa, tag, (pb & 0xffffu) | ((t0 + T2 + (pb >> 18)) << 16), tag}, rws, lane * 16, WS_PG + bid * 128, 16);
      }
      if (bid == 254) {
        until<4>(t0 + T2 + 300);
        float n, sdi, si2, sdt, sof;
#pragma unroll 1
        for (int k = 0; k < 4; ++k) {
          asm volatile("" ::: "memory");
          v4u g[4];
#pragma unroll
          for (int i = 0; i < 4; ++i) g[i] = LD128(rws, lane * 128, WS_PG + i * 8192, 16);
          n = sdi = si2 = sdt = sof = 0.f;
#pragma unroll
          for (int i = 0; i < 4; ++i) if (g[i][1] == tag && g[i][3] == tag) {
            n += 1.f; sdi += (float)(g[i][0] & 1023u); si2 += (float)((g[i][0] >> 10) & 1023u); sdt += (float)(g[i][2] & 0xffffu);
            sof += (float)(short)((g[i][2] >> 16) - (t0 + T2));
          }
          n = xrow_sum(row_sum(n));
          if (__builtin_amdgcn_readfirstlane(asu(n)) == asu(256.f)) break;
        }
        sdi = xrow_sum(row_sum(sdi)); si2 = xrow_sum(row_sum(si2)); sdt = xrow_sum(row_sum(sdt)); sof = xrow_sum(row_sum(sof));
        const float rn = __builtin_amdgcn_rcpf(n);
        const float te = sof * rn + (64.f - 0.25f * si2 * rn) * sdt * __builtin_amdgcn_rcpf(sdi) * EPS + (float)MARG;
        if (n >= 128.f && lane < 32) __builtin_amdgcn_raw_buffer_store_b64(v2u{(t0 + T2 + (unsigned)(int)te) | 1u, tag}, rws, lane * 256, WS_TE, 16);
      }
      until<4>(t0 + T2 + 600);
#pragma unroll 1
      for (int k = 0; k < 8; ++k) {
        asm volatile("" ::: "memory");
        const v2u g = __builtin_amdgcn_raw_buffer_load_b64(rws, 0, WS_TE + slot * 256, 16);
        if (__builtin_amdgcn_readfirstlane(g[1]) == tag) {
          LDSV(L_TE) = g[0];
          NOTE(10, __builtin_amdgcn_readfirstlane(g[0])); NOTE(11, clk()); NOTE(12, k + 1);
          break;
        }
        __builtin_amdgcn_s_sleep(16);
      }
    }
    bar();
    {
      const v4f fa = *(LAS v4f*)(sm + L_FA);
      const v2f p0 = *(LAS v2f*)(sm + L_PART + lane * 8), p1 = *(LAS v2f*)(sm + L_PART + 512 + lane * 8);
      const v2f p2 = *(LAS v2f*)(sm + L_PART + 1024 + lane * 8), p3 = *(LAS v2f*)(sm + L_PART + 1536 + lane * 8);
      const unsigned op = pk((fa[0] * p0[0] + fa[1] * p1[0]) + (fa[2] * p2[0] + fa[3] * p3[0]), (fa[0] * p0[1] + fa[1] * p1[1]) + (fa[2] * p2[1] + fa[3] * p3[1]));
      float za[8] = {0.f, 0.f, 0.f, 0.f, 0.f, 0.f, 0.f, 0.f};
#define ZSTEP(U) { const unsigned ob = dppu<BCAST + U>(op); _Pragma("unroll") for (int j = 0; j < 8; ++j) za[j] = dot2(WO[U][j], ob, za[j]); }
      ZSTEP(0) ZSTEP(1) ZSTEP(2) ZSTEP(3) ZSTEP(4) ZSTEP(5) ZSTEP(6) ZSTEP(7)
      ZSTEP(8) ZSTEP(9) ZSTEP(10) ZSTEP(11) ZSTEP(12) ZSTEP(13) ZSTEP(14) ZSTEP(15)
#undef ZSTEP
      const v2f z2 = tr_reduce(za);
      __builtin_amdgcn_raw_buffer_store_b128(v4u{asu(z2[0]), tag, asu(z2[1]), tag}, rws,
          (c >> 1) * 8192 + (c & 1) * 64 + r * 16, WS_Z + (b * 32 + hw * 8) * 8192 + (h * 2 + side) * 128, 16);
    }
    STAMP(3);
  }

  if (isGU) {
    {
      const int mat = w & 1, oc = lane & 3, rho = (lane >> 2) & 7, rd0 = lane >> 5;
      const __amdgpu_buffer_rsrc_t rg = RSRC(mat ? wu : wg, D * F * 2);
      const int go = ((rd0 * 64 + rho * 2) * F + oc * 8) * 2, gs = ((w >> 1) * 128 * F + fg * 32) * 2;
      v4u t[8];
#pragma unroll
      for (int j = 0; j < 8; ++j) t[j] = LD128(rg, go, gs + ((j >> 1) * 16 + (j & 1)) * F * 2, GAUX);
      const int kap = (w >> 1) * 16 + rd0 * 8 + rho;
      LAS char* dst = sm + L_FR + oc * FRT + ((kap >> 2) * 64 + (kap & 3) * 16 + mat) * 16;
#pragma unroll
      for (int f = 0; f < 8; ++f) *(LAS v4u*)(dst + f * 32) = tcol(t, f);
    }
    bar();
    STAMP(7);
    if (w >= 4) {
      const int hv = WS_H + (fg % HREP) * 8192 + (w - 4) * 2048 + lane * 16;
      GATE(GB);
      v4u g[BNS][2]; unsigned hvl[4];
      const int spin = ppoll<2, BNS>(rsw, hv, tag, g, hvl);
      STAMP(4); NOTE(9, spin + 1);
      const v2f st = ln_stats(v4u{hvl[0], hvl[1], hvl[2], hvl[3]});
      LAS char* hd = sm + L_HN + (w - 4) * 1088 + r * 128 + (lane & 3) * 32 + (c >> 2) * 4;
      *(LAS unsigned*)(hd) = ln_apply(hvl[0], st); *(LAS unsigned*)(hd + 16) = ln_apply(hvl[1], st);
      *(LAS unsigned*)(hd + 512) = ln_apply(hvl[2], st); *(LAS unsigned*)(hd + 528) = ln_apply(hvl[3], st);
      bar();
      keep(g);
    } else {
      v4u fr[16], hb[16];
#pragma unroll
      for (int s = 0; s < 16; ++s) fr[s] = *(LAS v4u*)(sm + L_FR + w * FRT + (s * 64 + lane) * 16);
      bar();
#pragma unroll
      for (int s = 0; s < 16; ++s) hb[s] = *(LAS v4u*)(sm + L_HN + (c & 3) * 1088 + s * 64 + r * 16);
      SB();
      v4f acc = {0.f, 0.f, 0.f, 0.f}, ac2 = {0.f, 0.f, 0.f, 0.f};
#pragma unroll
      for (int s = 0; s < 8; ++s) { acc = MFMA(fr[2 * s], hb[2 * s], acc); ac2 = MFMA(fr[2 * s + 1], hb[2 * s + 1], ac2); }
      acc += ac2;
      const unsigned gg = pk(acc[0], acc[2]), uu = pk(acc[1], acc[3]);
      const unsigned sg = pk(__builtin_amdgcn_rcpf(1.f + __builtin_amdgcn_exp2f(-L2E * blo(gg))), __builtin_amdgcn_rcpf(1.f + __builtin_amdgcn_exp2f(-L2E * bhi(gg))));
      const unsigned hh = pk(blo(sg) * blo(uu), bhi(sg) * bhi(uu));
      auto s2 = __builtin_amdgcn_permlane16_swap(hh, hh, false, false);
      const int o = fg * 4 + w, line = (o >> 4) * 16 + (o & 1) * 8 + ((o >> 1) & 7), rep = (r & 1) * 4 + (c >> 2);
      if (rep < GREP) __builtin_amdgcn_raw_buffer_store_b128(v4u{s2[0], tag, s2[1], tag}, rws, rep * 32768 + (c & 3) * 16 + (r >> 1) * 64, WS_HH + line * 128, 16);
      STAMP(5);
    }
    FLUSH(2);
  } else if (isDN) {
    if (w < 7) {
      {
        const int kap = lane >> 1, oc = lane & 1;
        const __amdgpu_buffer_rsrc_t rg = RSRC(wd, D * F * 2);
        const int go = (((kap >> 4) * 128 + (kap & 3) * 32 + ((kap >> 3) & 1) * 16 + ((kap >> 2) & 1) * 4) * D + oc * 8) * 2, gs = (w * 256 * D + cgD * 16) * 2;
        v4u t[8];
#pragma unroll
        for (int j = 0; j < 8; ++j) t[j] = LD128(rg, go, gs + ((j & 3) + (j >> 2) * 8) * D * 2, DAUX);
        LAS char* dst = sm + L_FR + w * 8704 + (kap >> 2) * 1088 + (kap & 3) * 272 + oc * 128;
#pragma unroll
        for (int f = 0; f < 8; ++f) *(LAS v4u*)(dst + f * 16) = tcol(t, f);
        asm volatile("s_waitcnt lgkmcnt(0)" ::: "memory");
      }
      STAMP(7);
      v4u fr[8];
#pragma unroll
      for (int s = 0; s < 8; ++s) fr[s] = *(LAS v4u*)(sm + L_FR + w * 8704 + s * 1088 + r * 272 + c * 16);
      const int hv = WS_HH + (cgD % GREP) * 32768 + w * 4096 + lane * 16;
      GATE(GC);
      v4u g[CNS][4]; unsigned hvl[8];
      const int spin = ppoll<4, CNS>(rsw, hv, tag, g, hvl);
      STAMP(4); NOTE(9, spin + 1);
      const v4u b0 = {hvl[0], hvl[1], hvl[2], hvl[3]}, b1 = {hvl[4], hvl[5], hvl[6], hvl[7]};
      v4f ac[4];
#pragma unroll
      for (int i = 0; i < 4; ++i) ac[i] = MFMA(fr[i], b0, (v4f{0.f, 0.f, 0.f, 0.f}));
#pragma unroll
      for (int i = 0; i < 4; ++i) ac[i] = MFMA(fr[4 + i], b1, ac[i]);
      v4f sum;
#pragma unroll
      for (int k = 0; k < 4; ++k) sum[k] = (ac[0][k] + dpp<SHL + 4>(ac[1][k])) + (dpp<SHL + 8>(ac[2][k]) + dpp<SHL + 12>(ac[3][k]));
      if (c < 4) *(LAS v4f*)(sm + L_RED + w * 256 + (r * 4 + c) * 16) = sum;
      bar();
      keep(g);
    } else {
      const int hv = WS_H + ((NG + cgD) % HREP) * 8192 + (c & 3) * 2048 + (cgD * 8 + r * 2 + (c >> 3)) * 8;
      v2u g;
#pragma unroll 1
      for (int spin = 0; spin < SPIN_CAP; ++spin) {
        asm volatile("" ::: "memory");
        g = __builtin_amdgcn_raw_buffer_load_b64(rws, hv, 0, 16);
        if (__all(g[1] == tag)) break;
        __builtin_amdgcn_s_sleep(4);
      }
      const float res = (c & 4) ? bhi(g[0]) : blo(g[0]);
      bar();
      float s = 0.f;
#pragma unroll
      for (int i = 0; i < 7; ++i) s += *(LAS float*)(sm + L_RED + i * 256 + (r * 4 + (c & 3)) * 16 + (c >> 2) * 4);
      const unsigned o = pk(res + rbf(s), 0.f);
      out[(c & 3) * D + cgD * 16 + r * 4 + (c >> 2)] = (u16)o;
      if (cgD == 0 && lane == 0) __builtin_amdgcn_raw_buffer_store_b32(tag, rws, 0, 0, 16);
      STAMP(5);
    }
    FLUSH(3);
  } else {
    FLUSH(isRED ? 1 : 4);
  }
}

extern "C" void kernel_launch(void* const* d_in, const int* in_sizes, int n_in, void* d_out, int out_size, void* d_ws, size_t ws_size, hipStream_t stream) {
  fused_block<<<256, 512, 0, stream>>>((const u16*)d_in[0], (const u16*)d_in[1], (const u16*)d_in[2], (const u16*)d_in[3], (const u16*)d_in[4],
                                       (const u16*)d_in[6], (const u16*)d_in[7], (const u16*)d_in[8], (u16*)d_out, (char*)d_ws);
}
```

```cpp
#include <hip/hip_runtime.h>
#include <hip/hip_bf16.h>
#include <cstdint>

typedef unsigned short u16;
typedef unsigned v4u __attribute__((ext_vector_type(4)));
typedef unsigned v2u __attribute__((ext_vector_type(2)));
typedef float v4f __attribute__((ext_vector_type(4)));
typedef float v2f __attribute__((ext_vector_type(2)));
typedef __bf16 bf16x2 __attribute__((ext_vector_type(2)));
typedef __bf16 bf16x8 __attribute__((ext_vector_type(8)));
#define LAS __attribute__((address_space(3)))

#define PRE 3
#define PACE 8
#define HREP 16
#define GAUX 2
#define DAUX 2
#define STG 7
#define T1 2400
#define T2 3600
#define MARG 190
#define RLG 350
#define GA 150
#define QG 340
#define KAUX 2
#define GC 150
#define KS 8
#define KN 2
#define EVA 16

constexpr int D = 512, F = 1792, NG = 56;
constexpr int NIT = 64;
constexpr int WS_ML = 65536;
constexpr int WS_PG = 131072;
constexpr int WS_TE = 196608;
constexpr int WS_H = 262144;
constexpr int WS_Q = 1048576;
constexpr int WS_Z = WS_Q, WS_O = WS_Q;
constexpr int WS_BYTES = WS_Q + 3 * 524288;
constexpr int SPIN_CAP = 400000;
constexpr int L_FR = 0;
constexpr int FRT = 16384 + 16;
constexpr int L_SC = 4 * FRT;
constexpr int L_QP = L_SC + 16384;
constexpr int L_PART = L_QP + 256;
constexpr int L_ML = L_PART + 1088;
constexpr int L_FA = L_ML + 32;
constexpr int L_TAG = L_FA + 16;
constexpr int L_TE = L_TAG + 4, L_PA = L_TAG + 8, L_PB = L_TAG + 12, L_CNT = L_TAG + 16;
constexpr int L_ST = L_TAG + 32;
constexpr int L_HN = L_TAG + 64;
constexpr int L_HH = L_HN + 4160;
constexpr int L_END = L_HH + 256;

constexpr float L2E = 1.4426950408889634f;

__device__ __forceinline__ float asf(unsigned u) { return __builtin_bit_cast(float, u); }
__device__ __forceinline__ unsigned asu(float f) { return __builtin_bit_cast(unsigned, f); }
__device__ __forceinline__ float blo(unsigned u) { return asf(u << 16); }
__device__ __forceinline__ float bhi(unsigned u) { return asf(u & 0xffff0000u); }
__device__ __forceinline__ unsigned pk(float a, float b) {
  bf16x2 r; r[0] = (__bf16)a; r[1] = (__bf16)b; return __builtin_bit_cast(unsigned, r);
}
__device__ __forceinline__ float rbf(float f) { __bf16 h = (__bf16)f; return (float)h; }
__device__ __forceinline__ float dot2(unsigned a, unsigned b, float acc) {
  return __builtin_amdgcn_fdot2_f32_bf16(__builtin_bit_cast(bf16x2, a), __builtin_bit_cast(bf16x2, b), acc, false);
}
template <int CTRL> __device__ __forceinline__ unsigned dppu(unsigned x) {
  return (unsigned)__builtin_amdgcn_mov_dpp((int)x, CTRL, 0xf, 0xf, true);
}
template <int CTRL> __device__ __forceinline__ float dpp(float x) { return asf(dppu<CTRL>(asu(x))); }
constexpr int XOR1 = 0xB1, XOR2 = 0x4E, HMIR = 0x141, ROR4 = 0x124, ROR8 = 0x128, SHL = 0x100;
__device__ __forceinline__ float row_sum(float x) {
  x += dpp<XOR1>(x); x += dpp<XOR2>(x); x += dpp<HMIR>(x); x += dpp<ROR8>(x); return x;
}
__device__ __forceinline__ float xrow_sum(float x) {
  auto s = __builtin_amdgcn_permlane16_swap(asu(x), asu(x), false, false); x = asf(s[0]) + asf(s[1]);
  auto t = __builtin_amdgcn_permlane32_swap(asu(x), asu(x), false, false); return asf(t[0]) + asf(t[1]);
}
__device__ __forceinline__ float xrow_max(float x) {
  auto s = __builtin_amdgcn_permlane16_swap(asu(x), asu(x), false, false); x = fmaxf(asf(s[0]), asf(s[1]));
  auto t = __builtin_amdgcn_permlane32_swap(asu(x), asu(x), false, false); return fmaxf(asf(t[0]), asf(t[1]));
}
__device__ __forceinline__ v2f tr_reduce(const float (&a)[8]) {
  float b[4];
#pragma unroll
  for (int j = 0; j < 4; ++j) { auto s = __builtin_amdgcn_permlane32_swap(asu(a[j]), asu(a[j + 4]), false, false); b[j] = asf(s[0]) + asf(s[1]); }
  v2f c;
#pragma unroll
  for (int j = 0; j < 2; ++j) { auto s = __builtin_amdgcn_permlane16_swap(asu(b[j]), asu(b[j + 2]), false, false); c[j] = asf(s[0]) + asf(s[1]); }
  return c;
}
__device__ __forceinline__ v2f ln_fin(float mean, float var) {
  const float sd = rbf(__builtin_amdgcn_sqrtf(rbf(rbf(var) + 1e-5f)));
  return v2f{rbf(mean), __builtin_amdgcn_rcpf(sd)};
}
__device__ __forceinline__ v2f ln_stats(const v2f (&in)[4]) {
  const float c0 = asf(__builtin_amdgcn_readfirstlane(asu(in[0][0])));
  v2f d[4];
#pragma unroll
  for (int j = 0; j < 4; ++j) d[j] = in[j] - c0;
  const v2f s = (d[0] + d[1]) + (d[2] + d[3]);
  const v2f q = __builtin_elementwise_fma(d[0], d[0], d[1] * d[1]) + __builtin_elementwise_fma(d[2], d[2], d[3] * d[3]);
  auto u = __builtin_amdgcn_permlane16_swap(asu(s[0] + s[1]), asu(q[0] + q[1]), false, false);
  float t = asf(u[0]) + asf(u[1]);
  auto v = __builtin_amdgcn_permlane32_swap(asu(t), asu(t), false, false);
  t = row_sum(asf(v[0]) + asf(v[1])) * (1.f / 512.f);
  const float md = asf(__builtin_amdgcn_readlane(asu(t), 0)), sq = asf(__builtin_amdgcn_readlane(asu(t), 16));
  return v2f{c0 + md, __builtin_amdgcn_rsqf(fmaxf(fmaf(-md, md, sq), 0.f) + 1e-5f)};
}
__device__ __forceinline__ unsigned ln_apply(unsigned in, v2f st) {
  const unsigned d = pk(blo(in) - st[0], bhi(in) - st[0]); return pk(blo(d) * st[1], bhi(d) * st[1]);
}
__device__ __forceinline__ unsigned xo(unsigned a, unsigned t, unsigned acc) { return __builtin_amdgcn_bitop3_b32(a, t, acc, 0xBE); }
#define NONE(p) (__builtin_amdgcn_ballot_w64(p) == 0)
__device__ __forceinline__ void bar() { asm volatile("s_waitcnt lgkmcnt(0)\n\ts_barrier" ::: "memory"); }
#define SB() __builtin_amdgcn_sched_barrier(0)
#define RSRC(p, n) __builtin_amdgcn_make_buffer_rsrc((void*)(p), 0, (int)(n), 0x00020000)
#define LD128(r, vo, so, aux) __builtin_amdgcn_raw_buffer_load_b128(r, vo, so, aux)
#define MFMA(a, b, c) __builtin_amdgcn_mfma_f32_16x16x32_bf16(__builtin_bit_cast(bf16x8, a), __builtin_bit_cast(bf16x8, b), c, 0, 0, 0)
#define LDSADD(off, v) __hip_atomic_fetch_add((LAS unsigned*)(sm + (off)), (unsigned)(v), __ATOMIC_RELAXED, __HIP_MEMORY_SCOPE_WORKGROUP)
#define LDSV(off) (*(volatile LAS unsigned*)(sm + (off)))
__device__ __forceinline__ unsigned clk() { return (unsigned)__builtin_amdgcn_s_memrealtime(); }
template <int N> __device__ __forceinline__ void until(unsigned t) {
#pragma unroll 1
  for (int k = 0; k < 4000; ++k) { if ((int)(clk() - t) >= 0) break; __builtin_amdgcn_s_sleep(N); }
}
#define LBL(n) ".Lwp" #n
#define PAD(n, next) asm volatile(LBL(n) ":\n\ts_cbranch_execz " LBL(next))
#define PADA(n, next) asm volatile("s_branch " LBL(n) "\n\t.p2align 12\n" LBL(n) ":\n\ts_cbranch_execz " LBL(next))
#define HOP(v, n) "s_cmp_eq_u32 %0, " #v "\n\ts_cbranch_scc1 " LBL(n) "\n\t"
#define TOUR(sel, hops, ret) asm volatile("s_mov_b64 exec, 0\n\t" hops LBL(ret) ":\n\ts_mov_b64 exec, -1" :: "s"(sel) : "scc")


#define PSET(a, b) "buffer_load_dwordx4 v[" #a ":" #a "+3], %9, %10, 0 offen sc1\n\tbuffer_load_dwordx4 v[" #b ":" #b "+3], %9, %10, 0 offen offset:1024 sc1\n\t"
#define PCHK(a, b) "s_waitcnt vmcnt(2)\n\tv_mov_b32 v248, v[" #a "]\n\tv_mov_b32 v249, v[" #a "+2]\n\tv_mov_b32 v250, v[" #b "]\n\tv_mov_b32 v251, v[" #b "+2]\n\t" \
  "v_xor_b32 v[" #a "], %12, v[" #a "+1]\n\tds_write_b128 %11, v[248:251]\n\tv_bitop3_b32 v[" #a "], v[" #a "+3], %12, v[" #a "] bitop3:0xbe\n\t" \
  "v_bitop3_b32 v[" #a "], v[" #b "+1], %12, v[" #a "] bitop3:0xbe\n\tv_bitop3_b32 v[" #a "], v[" #b "+3], %12, v[" #a "] bitop3:0xbe\n\t" \
  "v_cmp_ne_u32 vcc, 0, v[" #a "]\n\ts_cbranch_vccz 2f\n\t"
#define PUNP(lo, hi, r) "v_lshlrev_b32 %" #lo ", 16, v" #r "\n\tv_and_b32 %" #hi ", 0xffff0000, v" #r "\n\t"
__device__ __forceinline__ void ppoll(const v4u rs, int hv, int hl, unsigned tag, v2f (&val)[4], int& spins) {
  int n = SPIN_CAP;
  float e[8];
  asm volatile("s_waitcnt vmcnt(0)\n\t" PSET(232, 236) "s_sleep %13\n\t" PSET(240, 244)
               "1:\n\t" PCHK(232, 236) PSET(232, 236) PCHK(240, 244) PSET(240, 244)
               "s_sub_u32 %0, %0, 1\n\ts_cbranch_scc0 1b\n"
               "2:\n\t" PUNP(1, 2, 248) PUNP(3, 4, 249) PUNP(5, 6, 250) PUNP(7, 8, 251) "s_waitcnt lgkmcnt(0)\n\ts_barrier"
               : "+s"(n), "=v"(e[0]), "=v"(e[1]), "=v"(e[2]), "=v"(e[3]), "=v"(e[4]), "=v"(e[5]), "=v"(e[6]), "=v"(e[7])
               : "v"(hv), "s"(rs), "v"(hl), "s"(tag), "n"(STG)
               : "memory", "vcc", "scc", "v232", "v233", "v234", "v235", "v236", "v237", "v238", "v239", "v240", "v241", "v242", "v243", "v244", "v245",
                 "v246", "v247", "v248", "v249", "v250", "v251");
  spins = SPIN_CAP - n;
#pragma unroll
  for (int j = 0; j < 4; ++j) val[j] = v2f{e[2 * j], e[2 * j + 1]};
}
template <int NP> __device__ __forceinline__ v2u gather16(const __amdgpu_buffer_rsrc_t rws, int so, int lane, unsigned tag, v2u init, unsigned gate, int& spins) {
  const unsigned one = (lane & 1) ? 0x3f800000u : 0x3f80u, s0 = (lane & 2) ? 0u : one, s1 = (lane & 2) ? one : 0u;
  v4u sel = {s0, s0, s1, s1};
  const bool in = (lane & 12) == 0, on = lane < (NP - 48) * 4;
  v4f c0 = {in ? blo(init[0]) : 0.f, in ? bhi(init[0]) : 0.f, in ? blo(init[1]) : 0.f, in ? bhi(init[1]) : 0.f};
  int vz = (lane >> 3) * 16384 + (lane & 7) * 16, vl = on ? vz : WS_BYTES;
  unsigned tl = on ? tag : 0u;
  asm volatile("" : "+v"(sel), "+v"(c0), "+v"(vz), "+v"(vl), "+v"(tl));
  if (gate) until<1>(gate);
  if (NP == NG) { SB(); PAD(12, 102); SB(); }
  v4f d;
  int spin = 0;
#pragma unroll 1
  for (; spin < SPIN_CAP; ++spin) {
    asm volatile("" ::: "memory");
    v4u z[4];
#pragma unroll
    for (int j = 0; j < 4; ++j) z[j] = LD128(rws, j < 3 ? vz : vl, so + j * 131072, 16);
    SB();
    unsigned bad = xo(z[0][1], tag, z[0][3] ^ tag);
    SB();
    d = MFMA(sel, (v4u{z[0][0], z[1][0], z[0][2], z[1][2]}), c0);
    SB();
    bad = xo(z[2][1], tag, xo(z[2][3], tag, bad));
    SB();
    d = MFMA(sel, (v4u{z[2][0], z[3][0], z[2][2], z[3][2]}), d);
    SB();
    bad = xo(z[1][1], tag, xo(z[1][3], tag, xo(z[3][1], tl, xo(z[3][3], tl, bad))));
    asm volatile("" : "+v"(d));
    if (NONE(bad != 0)) break;
  }
  spins = spin;
  float e[4];
#pragma unroll
  for (int k = 0; k < 4; ++k) { e[k] = d[k] + dpp<ROR8>(d[k]); asm volatile("" : "+v"(e[k])); }
#pragma unroll
  for (int k = 0; k < 4; ++k) { e[k] += dpp<ROR4>(e[k]); asm volatile("" : "+v"(e[k])); }
  return v2u{pk(e[0], e[1]), pk(e[2], e[3])};
}

constexpr int LSTEP = 4096;
template <bool RE>
__device__ __forceinline__ void prock(v4u (&a)[8], const v4u q, float& mx, LAS char* dst, bool lo8, bool q1,
                                      const __amdgpu_buffer_rsrc_t rn, int vo, int so) {
  float x[8];
#pragma unroll
  for (int i = 0; i < 8; ++i) {
    x[i] = dot2(a[i][3], q[3], dot2(a[i][2], q[2], dot2(a[i][1], q[1], dot2(a[i][0], q[0], 0.f))));
    asm volatile("" : "+v"(x[i]));
    SB();
    if (RE) a[i] = LD128(rn, vo + (i & 3) * 1024, so + (i >> 2) * LSTEP, KAUX);
    SB();
  }
  float y[4], z[2];
#pragma unroll
  for (int j = 0; j < 4; ++j) { const float u = x[j] + dpp<ROR8>(x[j]), v = x[j + 4] + dpp<ROR8>(x[j + 4]); y[j] = lo8 ? u : v; }
#pragma unroll
  for (int j = 0; j < 2; ++j) { const float u = y[j] + dpp<HMIR>(y[j]), v = y[j + 2] + dpp<HMIR>(y[j + 2]); z[j] = q1 ? v : u; z[j] += dpp<XOR1>(z[j]); z[j] += dpp<XOR2>(z[j]); }
  const unsigned r01 = pk(z[0], z[1]);
  const float SC = 0.08838834764831845f;
  const unsigned t01 = pk(blo(r01) * SC, bhi(r01) * SC);
  const v2f s = {blo(t01), bhi(t01)};
  mx = fmaxf(fmaxf(s[0], s[1]), mx);
  *(LAS v2f*)dst = s;
  SB();
}
template <bool RE>
__device__ __forceinline__ void procv(v4u (&a)[8], const LAS char* src, float (&acc)[8], const __amdgpu_buffer_rsrc_t rn, int vo, int so) {
  const v4f p0 = *(const LAS v4f*)src, p1 = *(const LAS v4f*)(src + 16);
  SB();
#pragma unroll
  for (int i = 0; i < 8; ++i) {
    const float p = i < 4 ? p0[i & 3] : p1[i & 3];
#pragma unroll
    for (int k = 0; k < 4; ++k) { acc[2 * k] = fmaf(p, blo(a[i][k]), acc[2 * k]); acc[2 * k + 1] = fmaf(p, bhi(a[i][k]), acc[2 * k + 1]); }
    asm volatile("" : "+v"(acc[0]), "+v"(acc[1]), "+v"(acc[2]), "+v"(acc[3]), "+v"(acc[4]), "+v"(acc[5]), "+v"(acc[6]), "+v"(acc[7]));
    SB();
    if (RE) a[i] = LD128(rn, vo + (i & 3) * 1024, so + (i >> 2) * LSTEP, KAUX);
    SB();
  }
}

__device__ __forceinline__ v4u tcol(const v4u* t, int f) {
  const unsigned sel = (f & 1) ? 0x07060302u : 0x05040100u;
  v4u o;
#pragma unroll
  for (int j = 0; j < 4; ++j) o[j] = __builtin_amdgcn_perm(t[2 * j + 1][f >> 1], t[2 * j][f >> 1], sel);
  return o;
}

__global__ void __launch_bounds__(512, 2) __attribute__((aligned(4096)))
fused_block(const u16* __restrict__ x, const u16* __restrict__ kc, const u16* __restrict__ vc, const u16* __restrict__ wq,
            const u16* __restrict__ wo, char* ws, u16* out, const u16* __restrict__ wg, const u16* __restrict__ wu,
            const u16* __restrict__ wd) {
  __shared__ __attribute__((aligned(16))) char smem_[L_END > 81920 + 16 ? L_END : 81920 + 16];
  LAS char* const sm = (LAS char*)smem_;
  const int tid = threadIdx.x, lane = tid & 63;
  int w = __builtin_amdgcn_readfirstlane(tid >> 6);
  const int r = lane >> 4, c = lane & 15;
  const int bid = blockIdx.x, xg = (bid >> 1) & 3, slot = bid >> 3;
  const int xc = bid & 7, side = slot & 1, pr = slot >> 1;
  const int b = pr >> 2, bh = pr * 8 + xc, h = bh & 31;
  const bool isRED = bid & 1, isGU = !isRED && slot < NG / 4;
  const int fg = xg * (NG / 4) + slot;
  unsigned tag;
  const unsigned long long tl = __builtin_amdgcn_s_memrealtime();
  const int vo = lane * 16;
  const int sb = bh * (2 << 20) + (side * 4 + w - 4) * 8192;
  const int vx = (c >> 2) * 1024 + (r * 4 + (c & 3)) * 64, va = (c >> 2) * 1024 + (c & 1) * 64 + r * 16;
  const int vq = ((c >> 3) * 32 + r * 8) * 8192 + (c & 7) * 16, sq = b * (128 * 8192) + h * 256 + side * 128;
  asm volatile("" :: "s"(sb), "v"(vo), "v"(vx), "v"(va), "v"(vq), "s"(sq));
  TOUR(w, HOP(1, 1) HOP(2, 3) HOP(3, 4), 100);
  asm volatile("" :: "s"(tl));
  const unsigned t0 = (unsigned)tl;
  asm volatile("" :: "s"(x), "s"(kc), "s"(vc), "s"(wq), "s"(wo), "s"(ws));
  const __amdgpu_buffer_rsrc_t rws = RSRC(ws, WS_BYTES);
  const v4u rsw = {(unsigned)(uintptr_t)ws, (unsigned)((uintptr_t)ws >> 32), (unsigned)WS_BYTES, 0x00020000u};
  const __amdgpu_buffer_rsrc_t rk = RSRC(kc, 0x10000000);
#define SO(i) (sb + ((i) & 31) * 65536)
  v4u P[PRE + 1][8];
  unsigned ev1;
  if (w >= 4) {
#pragma unroll
    for (int i = 0; i < 8; ++i) {
      if (KS && i == KN) { SB(); __builtin_amdgcn_s_sleep(KS); SB(); }
      P[0][i] = LD128(rk, vo + (i & 3) * 1024, SO(0) + (i >> 2) * LSTEP, KAUX);
    }
#pragma unroll
    for (int u = 1; u <= PRE; ++u) {
#pragma unroll
      for (int i = 0; i < 8; ++i) {
        SB(); asm volatile("s_waitcnt vmcnt(%0)" :: "n"(PACE - 1)); SB();
        P[u][i] = LD128(rk, vo + (i & 3) * 1024, SO(u) + (i >> 2) * LSTEP, KAUX);
      }
    }
    SB();
  } else if (w == 1) ev1 = __builtin_amdgcn_raw_buffer_load_b32(rws, 0, 0, EVA);
  asm volatile("" : "+s"(w));
  if (w == 0) {
    const __amdgpu_buffer_rsrc_t rq = RSRC(wq, D * 4096 * 2);
    const __amdgpu_buffer_rsrc_t rx = RSRC(x, 4096);
    v4u (&W0)[8] = P[0], (&W1)[8] = P[1];
    v4u* const xs = &P[2][0];
    v4u* const xa = &P[2][4];
#pragma unroll
    for (int i = 0; i < 4; ++i) xs[i] = LD128(rx, vx + i * 16, 0, 0);
#pragma unroll
    for (int i = 0; i < 2; ++i) xa[i] = LD128(rx, va + i * 128, b * 256, 0);
    const unsigned ev = __builtin_amdgcn_raw_buffer_load_b32(rws, 0, 0, EVA);
    SB();
    PAD(1, 2);
    SB();
#pragma unroll
    for (int i = 0; i < 16; ++i) P[i >> 3][i & 7] = LD128(rq, vq, sq + ((i >> 3) * 64 + (i & 7)) * 8192, 2);
    SB();
    float s0 = 0.f, s1 = 0.f;
#pragma unroll
    for (int i = 0; i < 4; ++i) { s0 = dot2(xs[i][0], 0x3f803f80u, s0); s1 = dot2(xs[i][1], 0x3f803f80u, s1); s0 = dot2(xs[i][2], 0x3f803f80u, s0); s1 = dot2(xs[i][3], 0x3f803f80u, s1); }
    float sx = s0 + s1; sx += dpp<XOR1>(sx); sx += dpp<XOR2>(sx); sx = xrow_sum(sx);
    const float mean = sx * (1.f / 512.f);
    float v0 = 0.f, v1 = 0.f;
#pragma unroll
    for (int i = 0; i < 4; ++i)
#pragma unroll
      for (int k = 0; k < 4; ++k) { const float d0 = blo(xs[i][k]) - mean, d1 = bhi(xs[i][k]) - mean; v0 = fmaf(d0, d0, v0); v1 = fmaf(d1, d1, v1); }
    SB();
    PAD(2, 100);
    SB();
    float vs = v0 + v1; vs += dpp<XOR1>(vs); vs += dpp<XOR2>(vs); vs = xrow_sum(vs);
    const v2f st = ln_fin(mean, vs * (1.f / 512.f));
#pragma unroll
    for (int i = 0; i < 2; ++i)
#pragma unroll
      for (int k = 0; k < 4; ++k) xa[i][k] = ln_apply(xa[i][k], st);
    tag = __builtin_amdgcn_readfirstlane(ev) + 1u;
    v4f qa[8];
#pragma unroll
    for (int f = 0; f < 8; ++f) qa[f] = MFMA(xa[0], tcol(W0, f), (v4f{0.f, 0.f, 0.f, 0.f}));
    SB();
    PAD(3, 5);
    SB();
#pragma unroll
    for (int f = 0; f < 8; ++f) qa[f] = MFMA(xa[1], tcol(W1, f), qa[f]);
    float qv[4];
#pragma unroll
    for (int f = 0; f < 4; ++f) qv[f] = (c < 8 ? qa[f][0] : qa[f + 4][1]) + dpp<ROR8>(c < 8 ? qa[f + 4][0] : qa[f][1]);
#pragma unroll
    for (int t = 0; t < 2; ++t)
      __builtin_amdgcn_raw_buffer_store_b128(v4u{asu(qv[2 * t]), tag, asu(qv[2 * t + 1]), tag}, rws, r * 4096 + c * 16 + t * 256, WS_Q + h * 16384 + b * 1024 + side * 512, 16);
  }
  asm volatile("" : "+s"(w));

  if (w >= 4) {
    PADA(4, 6);
    const int sw = w - 4;
    const __amdgpu_buffer_rsrc_t rv = RSRC(vc, 0x10000000);
    bar();
    const v4u q = *(LAS v4u*)(sm + L_QP + c * 16);
    tag = *(LAS unsigned*)(sm + L_TAG);
    LAS char* const sc = sm + L_SC + sw * 4096;
    LAS char* const sd = sc + r * 32 + (c >> 2) * 8;
    const bool lo8 = c < 8, q1 = c & 4;
    float mx = -3.0e38f;
    prock<true>(P[0], q, mx, sd, lo8, q1, rk, vo, SO(PRE + 1));
    PAD(5, 7);
    prock<false>(P[1], q, mx, sd + 128, lo8, q1, rk, vo, 0);
    prock<false>(P[2], q, mx, sd + 256, lo8, q1, rk, vo, 0);
    PAD(6, 17);
    prock<false>(P[3], q, mx, sd + 384, lo8, q1, rk, vo, 0);
    PAD(7, 18);
    int tau = 0, due = 0, ph = 0, it1 = 0, t1 = 0;
    unsigned tev = 0;
    auto pace = [&](int it) __attribute__((always_inline)) {
      int now = (int)(clk() - t0);
      if (ph == 0) { if (now >= T1) { it1 = it; t1 = now; ph = 1; } }
      else if (ph == 1) {
        if (now >= T2) {
          if (lane == 0) { LDSADD(L_PB, (now - t1) | (min(now - T2, 4000) << 16)); LDSADD(L_PA, (it - it1) | (it << 10) | (1 << 28)); }
          ph = 2;
        }
      } else if (ph == 2) {
        const unsigned ta = __builtin_amdgcn_readfirstlane(tev);
        tev = LDSV(L_TE);
        if (ta) {
          const int te = (int)(ta - t0) - (isGU ? RLG : 0);
          tau = min(max(__builtin_amdgcn_readfirstlane((int)((float)((te - now) * 64) * __builtin_amdgcn_rcpf((float)(NIT - it)))), 1), 64 * 250);
          due = now * 64; ph = 3;
        }
      }
      if (ph == 3) {
#pragma unroll 1
        for (int k = 0; k < 200; ++k) {
          if (now * 64 >= due) break;
          __builtin_amdgcn_s_sleep(1);
          now = (int)(clk() - t0);
        }
        due += tau;
      }
    };
#pragma unroll 1
    for (int it = PRE + 2; it <= NIT / 2; ++it) {
      pace(it);
      prock<true>(P[0], q, mx, sd + (it - 1) * 128, lo8, q1, RSRC(it < NIT / 2 ? kc : vc, 0x10000000), vo, SO(it));
    }
    {
      SB();
      PADA(8, 9);
      SB();
      mx = fmaxf(mx, dpp<HMIR>(mx)); mx = fmaxf(mx, dpp<ROR8>(mx)); mx = xrow_max(mx);
      const float mL = mx * L2E;
      float l = 0.f;
#pragma unroll
      for (int j = 0; j < 4; ++j) {
        v4f s = *(LAS v4f*)(sc + j * 1024 + lane * 16);
#pragma unroll
        for (int k = 0; k < 4; ++k) s[k] = __builtin_amdgcn_exp2f(fmaf(s[k], L2E, -mL));
        l += (s[0] + s[1]) + (s[2] + s[3]);
        *(LAS v4f*)(sc + j * 1024 + lane * 16) = s;
      }
      l = xrow_sum(row_sum(l));
      if (lane == 0) { *(volatile LAS v2f*)(sm + L_ML + sw * 8) = v2f{mx, l}; LDSADD(L_CNT, 1); }
      asm volatile("" ::: "memory");
    }
    PAD(9, 10);
    const int col = xg * D + (slot >> 1) * 32 + (slot & 1) * 4 + (lane & 3) * 8;
    const v2u xv = *(const v2u*)(x + col);
    float acc[8] = {0.f, 0.f, 0.f, 0.f, 0.f, 0.f, 0.f, 0.f};
#pragma unroll 1
    for (int it = NIT / 2 + 1; it < NIT; ++it) {
      pace(it);
      procv<true>(P[0], sc + r * 32 + (it - NIT / 2 - 1) * 128, acc, rv, vo, SO(it));
    }
    PAD(10, 11);
    procv<false>(P[0], sc + r * 32 + (NIT / 2 - 1) * 128, acc, rv, vo, 0);
    { const v2f o2 = tr_reduce(acc); *(LAS unsigned*)(sm + L_PART + sw * 272 + c * 16 + r * 4) = pk(o2[0], o2[1]); }
    bar();
    tag = __builtin_amdgcn_readfirstlane(tag);
    if (isRED) {
      if (sw != 0) return;
      const int rb = xg, cg = slot;
      PADA(11, 12);
      const unsigned te = __builtin_amdgcn_readfirstlane(LDSV(L_TE));
      int hv = (lane >> 2) * 8192 + (lane & 3) * 16, ov = lane < 4 ? col * 2 : 4096, hs = WS_H + rb * 2048 + cg * 64, spin;
      unsigned tv = tag;
      asm volatile("" : "+v"(hv), "+v"(tv), "+v"(ov), "+s"(hs));
      const v2u hp = gather16<64>(rws, WS_Z + rb * 4096 + cg * 128, lane, tag + 1u, xv, te ? te + GA : 0u, spin);
      __builtin_amdgcn_raw_buffer_store_b128(v4u{hp[0], tv, hp[1], tv}, rws, hv, hs, 16);
      SB();
      const unsigned tha = clk();
      const v2u dn = gather16<NG>(rws, WS_O + rb * 4096 + cg * 128, lane, tag + 2u, hp, (tha + GC) | 1u, spin);
      __builtin_amdgcn_raw_buffer_store_b64(dn, RSRC(out, 4096), ov, 0, 0);
      SB();
      if (bid == 1 && lane == 0) __builtin_amdgcn_raw_buffer_store_b32(tag + 2u, rws, 0, 0, 16);
      __builtin_amdgcn_endpgm();
    }
  }
  asm volatile("" : "+s"(w));
  if (w < 4) {
    const int hw = w;
    PAD(18, 100);
    if (hw == 1) {
      tag = __builtin_amdgcn_readfirstlane(ev1) + 1u;
      v4u g[4];
      const int qo = WS_Q + h * 16384 + b * 4096;
      if (QG) until<1>(t0 + QG);
#pragma unroll 1
      for (int spin = 0; spin < SPIN_CAP; ++spin) {
        asm volatile("" ::: "memory");
#pragma unroll
        for (int j = 0; j < 4; ++j) g[j] = LD128(rws, lane * 16 + j * 1024, qo, 16);
        unsigned bad = 0;
#pragma unroll
        for (int j = 0; j < 4; ++j) bad |= (g[j][1] ^ tag) | (g[j][3] ^ tag);
        if (__all(bad == 0)) break;
      }
      const float q0 = (asf(g[0][0]) + asf(g[1][0])) + (asf(g[2][0]) + asf(g[3][0]));
      const float q1 = (asf(g[0][2]) + asf(g[1][2])) + (asf(g[2][2]) + asf(g[3][2]));
      *(LAS unsigned*)(sm + L_QP + ((r >> 1) * 32 + (c & 7) * 4 + (c >> 3) * 2 + (r & 1)) * 4) = pk(q0, q1);
      if (lane < 16) *(LAS unsigned*)(sm + L_TAG + lane * 4) = lane ? 0u : tag;
    }
    bar();
    tag = __builtin_amdgcn_readfirstlane(*(LAS unsigned*)(sm + L_TAG));
    v4u WO[4][8];
    {
      const __amdgpu_buffer_rsrc_t ro = RSRC(wo, D * 4096 * 2);
      const int vo = r * 8192 + c * 16, so = h * 131072 + hw * 256;
#pragma unroll
      for (int u = 0; u < 16; ++u) {
        const v4u t0 = LD128(ro, vo, so + (u >> 2) * 32768 + (u & 3) * 2048, 2), t1 = LD128(ro, vo + 1024, so + (u >> 2) * 32768 + (u & 3) * 2048, 2);
#pragma unroll
        for (int f = 0; f < 8; ++f) {
          unsigned p = __builtin_amdgcn_perm(t1[f >> 1], t0[f >> 1], (f & 1) ? 0x07060302u : 0x05040100u);
          asm volatile("" : "+v"(p));
          WO[u >> 2][f][u & 3] = p;
        }
        SB();
        if (u == 3) PAD(17, 100);
      }
    }
    TOUR(w, HOP(0, 8) HOP(1, 13), 102);
    if (hw == 2) {
#pragma unroll 1
      for (int spin = 0; spin < SPIN_CAP; ++spin) { if (__builtin_amdgcn_readfirstlane(LDSV(L_CNT)) == 4) break; __builtin_amdgcn_s_sleep(8); }
      asm volatile("" ::: "memory");
      const v2f ml = *(volatile LAS v2f*)(sm + L_ML + (lane & 3) * 8);
      float M = fmaxf(ml[0], dpp<XOR1>(ml[0])); M = fmaxf(M, dpp<XOR2>(M));
      float L = ml[1] * __builtin_amdgcn_exp2f((ml[0] - M) * L2E); L += dpp<XOR1>(L); L += dpp<XOR2>(L);
      if (lane < 8) __builtin_amdgcn_raw_buffer_store_b128(v4u{asu(M), tag, asu(L), tag}, rws, lane * 16, WS_ML + bid * 128, 16);
      v4u g;
#pragma unroll 1
      for (int spin = 0; spin < SPIN_CAP; ++spin) {
        __builtin_amdgcn_s_sleep(64);
        asm volatile("" ::: "memory");
        g = LD128(rws, 0, WS_ML + (bid ^ 8) * 128, 16);
        if (__all(g[1] == tag && g[3] == tag)) break;
      }
      const float Mo = asf(g[0]), Mt = fmaxf(M, Mo);
      const float La = L * __builtin_amdgcn_exp2f((M - Mt) * L2E), Lb = asf(g[2]) * __builtin_amdgcn_exp2f((Mo - Mt) * L2E);
      if (lane < 4) *(LAS float*)(sm + L_FA + lane * 4) = __builtin_amdgcn_exp2f((ml[0] - Mt) * L2E) / (La + Lb);
    }
    if (hw == 3) {
      until<8>(t0 + T2);
      unsigned pa = 0;
#pragma unroll 1
      for (int k = 0; k < 100; ++k) { pa = __builtin_amdgcn_readfirstlane(LDSV(L_PA)); if ((pa >> 28) == 4) break; __builtin_amdgcn_s_sleep(2); }
      if ((pa >> 28) == 4) {
        const unsigned pb = LDSV(L_PB);
        if (lane < 8) __builtin_amdgcn_raw_buffer_store_b128(v4u{pa, tag, (pb & 0xffffu) | ((t0 + T2 + (pb >> 18)) << 16), tag}, rws, lane * 16, WS_PG + bid * 128, 16);
      }
      if (bid == 254) {
        until<4>(t0 + T2 + 300);
        float n, sdi, si2, sdt, sof;
#pragma unroll 1
        for (int k = 0; k < 4; ++k) {
          asm volatile("" ::: "memory");
          v4u g[4];
#pragma unroll
          for (int i = 0; i < 4; ++i) g[i] = LD128(rws, lane * 128, WS_PG + i * 8192, 16);
          n = sdi = si2 = sdt = sof = 0.f;
#pragma unroll
          for (int i = 0; i < 4; ++i) if (g[i][1] == tag && g[i][3] == tag) {
            n += 1.f; sdi += (float)(g[i][0] & 1023u); si2 += (float)((g[i][0] >> 10) & 1023u); sdt += (float)(g[i][2] & 0xffffu);
            sof += (float)(short)((g[i][2] >> 16) - (t0 + T2));
          }
          n = xrow_sum(row_sum(n));
          if (__builtin_amdgcn_readfirstlane(asu(n)) == asu(256.f)) break;
        }
        sdi = xrow_sum(row_sum(sdi)); si2 = xrow_sum(row_sum(si2)); sdt = xrow_sum(row_sum(sdt)); sof = xrow_sum(row_sum(sof));
        const float rn = __builtin_amdgcn_rcpf(n);
        const float te = sof * rn + (64.f - 0.25f * si2 * rn) * sdt * __builtin_amdgcn_rcpf(sdi) + (float)MARG;
        if (n >= 128.f && lane < 32) __builtin_amdgcn_raw_buffer_store_b64(v2u{(t0 + T2 + (unsigned)(int)te) | 1u, tag}, rws, lane * 256, WS_TE, 16);
      }
      until<4>(t0 + T2 + 600);
#pragma unroll 1
      for (int k = 0; k < 8; ++k) {
        asm volatile("" ::: "memory");
        const v2u g = __builtin_amdgcn_raw_buffer_load_b64(rws, 0, WS_TE + slot * 256, 16);
        if (__builtin_amdgcn_readfirstlane(g[1]) == tag) {
          LDSV(L_TE) = g[0];
          break;
        }
        __builtin_amdgcn_s_sleep(16);
      }
    }
    {
      int zv = ((c >> 2) * 2 + r) * 128 + (c & 3) * 16 + (r < 2 ? 0 : WS_BYTES), pv = L_PART + (c & 3) * 272 + r * 16;
      const int zs = WS_Z + h * 16384 + b * 4096 + hw * 1024 + side * 64;
      unsigned tz = tag + 1u;
      asm volatile("" : "+v"(zv), "+v"(pv), "+v"(tz));
      bar();
      v4u pa[4];
#pragma unroll
      for (int s = 0; s < 4; ++s) pa[s] = *(LAS v4u*)(sm + pv + s * 64);
      const v4f fa = *(LAS v4f*)(sm + L_FA);
      SB();
      v4f a[8];
      float y[8];
      unsigned zp[4];
#pragma unroll
      for (int f = 0; f < 10; ++f) {
#pragma unroll
        for (int s = 0; s < 4; ++s) {
          const int e = f - 2 + (s >> 1), p = s & 1;
          if (f < 8) a[f] = MFMA(pa[s], WO[s][f], s ? a[f] : (v4f{0.f, 0.f, 0.f, 0.f}));
          SB();
          if (s < 2 && e >= 0) {
            y[e] = p ? fmaf(fa[3], a[e][3], fmaf(fa[2], a[e][2], y[e])) : fmaf(fa[1], a[e][1], fa[0] * a[e][0]);
            asm volatile("" : "+v"(y[e]));
          }
          if (s == 2 && f >= 3 && (f & 1)) {
            zp[f / 2 - 1] = pk(y[f - 3], y[f - 2]);
            asm volatile("" : "+v"(zp[f / 2 - 1]));
            if (f > 5) zp[f / 2 - 3] = (r & 1) ? zp[f / 2 - 1] : zp[f / 2 - 3];
          }
          SB();
        }
      }
      __builtin_amdgcn_raw_buffer_store_b128(v4u{zp[0], tz, zp[1], tz}, rws, zv, zs, 16);
    }
  }

  if (isGU) {
    v4u td[8];
    SB();
    PADA(13, 14);
    SB();
    {
      const int mat = w & 1, oc = lane & 3, rho = (lane >> 2) & 7, rd0 = lane >> 5;
      const __amdgpu_buffer_rsrc_t rg = RSRC(mat ? wu : wg, D * F * 2);
      const int go = ((rd0 * 32 + (rho & 3) * 8 + (rho & 4)) * F + oc * 8) * 2, gs = ((w >> 1) * 64 * F + fg * 32) * 2;
      v4u t[8];
#pragma unroll
      for (int j = 0; j < 8; ++j) t[j] = LD128(rg, go, gs + ((j & 3) + (j >> 2) * 256) * F * 2, GAUX);
      if (w >= 4) {
        const __amdgpu_buffer_rsrc_t rd = RSRC(wd, D * F * 2);
#pragma unroll
        for (int j = 0; j < 8; ++j) td[j] = LD128(rd, r * 8192 + c * 16, (fg * 32 + j) * 1024 + (w - 4) * 256, DAUX);
      }
      const int kap = (w >> 1) * 16 + rd0 * 8 + rho;
      LAS char* dst = sm + L_FR + oc * FRT + ((kap >> 2) * 64 + (kap & 3) * 16 + mat) * 16;
#pragma unroll
      for (int f = 0; f < 8; ++f) *(LAS v4u*)(dst + f * 32) = tcol(t, f);
    }
    bar();
    if (w >= 4) {
      v4u fd[8];
#pragma unroll
      for (int f = 0; f < 8; ++f) { fd[f] = tcol(td, f); asm volatile("" : "+v"(fd[f])); }
      const int hv = WS_H + (fg % HREP) * 8192 + (w - 4) * 2048 + lane * 16;
      int ha_ = L_HH + (c >> 2) * 64 + r * 16, ov = r * 4096 + (c >> 2) * 256 + (c & 3) * 16, os = WS_O + (fg >> 1) * 16384 + (w - 4) * 1024 + (fg & 1) * 64, spin;
      v4u o0 = {0u, tag + 2u, 0u, tag + 2u}, o1 = o0;
      int sv = L_ST + (w - 4) * 8;
      asm volatile("" : "+v"(ha_), "+v"(ov), "+s"(os), "+v"(o0), "+v"(o1), "+v"(sv));
      PAD(15, 16);
      v2f hv4[4];
      ppoll(rsw, hv, L_HN + (w - 4) * 1040 + lane * 16 + (int)(uintptr_t)sm, tag, hv4, spin);
      const v2f st = ln_stats(hv4);
      *(volatile LAS v2u*)(sm + sv) = v2u{asu(st[0]) | 1u, asu(st[1])};
      PAD(16, 102);
      bar();
      const v4u ha = *(LAS v4u*)(sm + ha_);
      v4f o[8];
#pragma unroll
      for (int f = 0; f < 8; ++f) {
        o[f] = MFMA(ha, fd[f], (v4f{0.f, 0.f, 0.f, 0.f}));
        SB();
        if (f == 4) o0[0] = pk(o[0][0], o[1][0]);
        if (f == 6) { o0[2] = pk(o[2][0], o[3][0]); __builtin_amdgcn_raw_buffer_store_b128(o0, rws, ov, os, 16); }
        if (f == 7) o1[0] = pk(o[4][0], o[5][0]);
        SB();
      }
      o1[2] = pk(o[6][0], o[7][0]);
      __builtin_amdgcn_raw_buffer_store_b128(o1, rws, ov + 128, os, 16);
    } else {
      v4u fr[16], hb[4];
#pragma unroll
      for (int s = 0; s < 16; ++s) fr[s] = *(LAS v4u*)(sm + L_FR + w * FRT + (s * 64 + lane) * 16);
      v4f cs = {0.f, 0.f, 0.f, 0.f};
#pragma unroll
      for (int s = 0; s < 16; ++s) cs = MFMA(fr[s], (v4u{0x3f803f80u, 0x3f803f80u, 0x3f803f80u, 0x3f803f80u}), cs);
      int sa = L_ST + (c & 3) * 8 + (int)(uintptr_t)sm, hw_ = c < 4 ? L_HH + c * 64 + w * 16 + r * 4 : L_SC + tid * 4;
      asm volatile("" : "+v"(cs), "+v"(sa), "+v"(hw_));
      PAD(14, 15);
      bar();
#pragma unroll
      for (int u = 0; u < 4; ++u) hb[u] = *(LAS v4u*)(sm + L_HN + (c & 3) * 1040 + (c >> 2) * 64 + r * 16 + u * 256);
      SB();
      v4f ac[4];
#pragma unroll
      for (int i = 0; i < 4; ++i) ac[i] = MFMA(fr[i], hb[0], (v4f{0.f, 0.f, 0.f, 0.f}));
#pragma unroll
      for (int s = 4; s < 16; ++s) ac[s & 3] = MFMA(fr[s], hb[s >> 2], ac[s & 3]);
      SB();
      v2u st;
      asm volatile("ds_read_b64 %0, %1" : "=v"(st) : "v"(sa) : "memory");
      SB();
      float acc[4];
#pragma unroll
      for (int k = 0; k < 4; ++k) { acc[k] = ac[0][k] + dpp<SHL + 4>(ac[1][k]); asm volatile("" : "+v"(acc[k])); }
#pragma unroll
      for (int k = 0; k < 4; ++k) { acc[k] += dpp<SHL + 8>(ac[2][k]); asm volatile("" : "+v"(acc[k])); }
#pragma unroll
      for (int k = 0; k < 4; ++k) { acc[k] += dpp<SHL + 12>(ac[3][k]); asm volatile("" : "+v"(acc[k])); }
      asm volatile("1:\n\ts_waitcnt lgkmcnt(0)\n\tv_cmp_eq_u64 vcc, 0, %0\n\ts_cbranch_vccz 2f\n\tds_read_b64 %0, %1\n\ts_branch 1b\n2:" : "+v"(st) : "v"(sa) : "memory", "vcc");
      const float mean = asf(st[0]), rs = asf(st[1]), kg = -L2E * rs;
#pragma unroll
      for (int k = 0; k < 4; ++k) { acc[k] = fmaf(-mean, cs[k], acc[k]) * ((k & 1) ? rs : kg); asm volatile("" : "+v"(acc[k])); }
      const float h0 = acc[1] * __builtin_amdgcn_rcpf(1.f + __builtin_amdgcn_exp2f(acc[0])), h1 = acc[3] * __builtin_amdgcn_rcpf(1.f + __builtin_amdgcn_exp2f(acc[2]));
      *(LAS unsigned*)(sm + hw_) = pk(h0, h1);
      bar();
    }
  }
}

extern "C" void kernel_launch(void* const* d_in, const int* in_sizes, int n_in, void* d_out, int out_size, void* d_ws, size_t ws_size, hipStream_t stream) {
  fused_block<<<256, 512, 0, stream>>>((const u16*)d_in[0], (const u16*)d_in[1], (const u16*)d_in[2], (const u16*)d_in[3], (const u16*)d_in[4],
                                       (char*)d_ws, (u16*)d_out, (const u16*)d_in[6], (const u16*)d_in[7], (const u16*)d_in[8]);
}
```

```cpp
#include <hip/hip_runtime.h>
#include <hip/hip_bf16.h>
#include <cstdint>

typedef unsigned short u16;
typedef unsigned v4u __attribute__((ext_vector_type(4)));
typedef unsigned v2u __attribute__((ext_vector_type(2)));
typedef float v4f __attribute__((ext_vector_type(4)));
typedef float v2f __attribute__((ext_vector_type(2)));
typedef __bf16 bf16x2 __attribute__((ext_vector_type(2)));
typedef __bf16 bf16x8 __attribute__((ext_vector_type(8)));
#define LAS __attribute__((address_space(3)))

#define PRE 4
#define PACE 8
#define GAUX 2
#define DAUX 2
#define STG 7
#define GA 50
#define QG 340
#define KAUX 2
#define GC 140
#define KS 8
#define KN 2
#define QD 75
#define OAUX 2
#define VTH -35.f
#define TN 110
#define TA 1200
#define MA 265
#define DC 150
#define DP 380
#define T3 5200
#define KF 16
#define LZ 62
#define KL 19
#define KB 50
#define LN 60
#define NK 36
#define NX 58
#define CX 205
#define KSL 2
#define WM ((h & 3) * 2 + (h >> 2 & 3) * 32)
#define SG 110
#define FW0 2700
#define FWD 640

constexpr int D = 512, F = 1792, NG = 56;
constexpr int NIT = 64;
constexpr int WS_ML = 65536;
constexpr int WS_PG = 786432;
constexpr int WS_TE = 196608;
constexpr int WS_H = 262144;
constexpr int WS_PR = 524288;
constexpr int WS_Q = 1048576;
constexpr int WS_Z = WS_Q, WS_O = WS_Q;
constexpr int WS_BYTES = WS_Q + 3 * 524288;
constexpr int SPIN_CAP = 400000;
constexpr int L_TAG = 0;
constexpr int L_LAG = 64;
constexpr int L_FR = 128;
constexpr int FRT = 16384 + 16;
constexpr int L_SC = L_FR + 4 * FRT;
constexpr int L_QP = L_SC + 16384;
constexpr int L_PART = L_QP + 256;
constexpr int L_ML = L_PART + 1088;
constexpr int L_FA = L_ML + 32;
constexpr int L_TE = L_TAG + 4, L_PA = L_TAG + 8, L_CNT = L_TAG + 16, L_FLG = L_TAG + 20;
constexpr int L_ST = L_TAG + 32;
constexpr int L_HN = L_FA + 16;
constexpr int L_HH = L_HN + 4160;
constexpr int L_FD = L_HH + 256;
constexpr int L_END = L_FD + 32768;

constexpr float L2E = 1.4426950408889634f;

__device__ __forceinline__ float asf(unsigned u) { return __builtin_bit_cast(float, u); }
__device__ __forceinline__ unsigned asu(float f) { return __builtin_bit_cast(unsigned, f); }
__device__ __forceinline__ float blo(unsigned u) { return asf(u << 16); }
__device__ __forceinline__ float bhi(unsigned u) { return asf(u & 0xffff0000u); }
__device__ __forceinline__ unsigned pk(float a, float b) {
  bf16x2 r; r[0] = (__bf16)a; r[1] = (__bf16)b; return __builtin_bit_cast(unsigned, r);
}
__device__ __forceinline__ float rbf(float f) { __bf16 h = (__bf16)f; return (float)h; }
__device__ __forceinline__ float dot2(unsigned a, unsigned b, float acc) {
  return __builtin_amdgcn_fdot2_f32_bf16(__builtin_bit_cast(bf16x2, a), __builtin_bit_cast(bf16x2, b), acc, false);
}
template <int CTRL> __device__ __forceinline__ unsigned dppu(unsigned x) {
  return (unsigned)__builtin_amdgcn_mov_dpp((int)x, CTRL, 0xf, 0xf, true);
}
template <int CTRL> __device__ __forceinline__ float dpp(float x) { return asf(dppu<CTRL>(asu(x))); }
constexpr int XOR1 = 0xB1, XOR2 = 0x4E, HMIR = 0x141, ROR4 = 0x124, ROR8 = 0x128, SHL = 0x100;
__device__ __forceinline__ float row_sum(float x) {
  x += dpp<XOR1>(x); x += dpp<XOR2>(x); x += dpp<HMIR>(x); x += dpp<ROR8>(x); return x;
}
__device__ __forceinline__ float xrow_sum(float x) {
  auto s = __builtin_amdgcn_permlane16_swap(asu(x), asu(x), false, false); x = asf(s[0]) + asf(s[1]);
  auto t = __builtin_amdgcn_permlane32_swap(asu(x), asu(x), false, false); return asf(t[0]) + asf(t[1]);
}
__device__ __forceinline__ float xrow_max(float x) {
  auto s = __builtin_amdgcn_permlane16_swap(asu(x), asu(x), false, false); x = fmaxf(asf(s[0]), asf(s[1]));
  auto t = __builtin_amdgcn_permlane32_swap(asu(x), asu(x), false, false); return fmaxf(asf(t[0]), asf(t[1]));
}
__device__ __forceinline__ v2f tr_reduce(const float (&a)[8]) {
  float b[4];
#pragma unroll
  for (int j = 0; j < 4; ++j) { auto s = __builtin_amdgcn_permlane32_swap(asu(a[j]), asu(a[j + 4]), false, false); b[j] = asf(s[0]) + asf(s[1]); }
  v2f c;
#pragma unroll
  for (int j = 0; j < 2; ++j) { auto s = __builtin_amdgcn_permlane16_swap(asu(b[j]), asu(b[j + 2]), false, false); c[j] = asf(s[0]) + asf(s[1]); }
  return c;
}
__device__ __forceinline__ v2f ln_fin(float mean, float var) {
  const float sd = rbf(__builtin_amdgcn_sqrtf(rbf(rbf(var) + 1e-5f)));
  return v2f{rbf(mean), __builtin_amdgcn_rcpf(sd)};
}
__device__ __forceinline__ v2f ln_stats(const v2f (&in)[4]) {
  const float c0 = asf(__builtin_amdgcn_readfirstlane(asu(in[0][0])));
  v2f d[4];
#pragma unroll
  for (int j = 0; j < 4; ++j) d[j] = in[j] - c0;
  const v2f s = (d[0] + d[1]) + (d[2] + d[3]);
  const v2f q = __builtin_elementwise_fma(d[0], d[0], d[1] * d[1]) + __builtin_elementwise_fma(d[2], d[2], d[3] * d[3]);
  auto u = __builtin_amdgcn_permlane16_swap(asu(s[0] + s[1]), asu(q[0] + q[1]), false, false);
  float t = asf(u[0]) + asf(u[1]);
  auto v = __builtin_amdgcn_permlane32_swap(asu(t), asu(t), false, false);
  t = row_sum(asf(v[0]) + asf(v[1])) * (1.f / 512.f);
  const float md = asf(__builtin_amdgcn_readlane(asu(t), 0)), sq = asf(__builtin_amdgcn_readlane(asu(t), 16));
  return v2f{c0 + md, __builtin_amdgcn_rsqf(fmaxf(fmaf(-md, md, sq), 0.f) + 1e-5f)};
}
__device__ __forceinline__ unsigned ln_apply(unsigned in, v2f st) {
  const unsigned d = pk(blo(in) - st[0], bhi(in) - st[0]); return pk(blo(d) * st[1], bhi(d) * st[1]);
}
__device__ __forceinline__ unsigned xo(unsigned a, unsigned t, unsigned acc) { return __builtin_amdgcn_bitop3_b32(a, t, acc, 0xBE); }
#define NONE(p) (__builtin_amdgcn_ballot_w64(p) == 0)
__device__ __forceinline__ void bar() { asm volatile("s_waitcnt lgkmcnt(0)\n\ts_barrier" ::: "memory"); }
#define SB() __builtin_amdgcn_sched_barrier(0)
#define RSRC(p, n) __builtin_amdgcn_make_buffer_rsrc((void*)(p), 0, (int)(n), 0x00020000)
#define LD128(r, vo, so, aux) __builtin_amdgcn_raw_buffer_load_b128(r, vo, so, aux)
#define MFMA(a, b, c) __builtin_amdgcn_mfma_f32_16x16x32_bf16(__builtin_bit_cast(bf16x8, a), __builtin_bit_cast(bf16x8, b), c, 0, 0, 0)
#define LDSADD(off, v) asm volatile("ds_add_u32 %0, %1 offset:%2" :: "v"((int)(uintptr_t)sm), "v"((unsigned)(v)), "n"(off) : "memory")
#define LDSV(off) (*(volatile LAS unsigned*)(sm + (off)))
__device__ __forceinline__ unsigned clk() { return (unsigned)__builtin_amdgcn_s_memrealtime(); }
template <int N> __device__ __forceinline__ void until(unsigned t) {
#pragma unroll 1
  for (int k = 0; k < 4000; ++k) { if ((int)(clk() - t) >= 0) break; __builtin_amdgcn_s_sleep(N); }
}
#define LBL(n) ".Lwp" #n
#define PAD(n, next) asm volatile(LBL(n) ":\n\ts_cbranch_execz " LBL(next))
#define PADA(n, next) asm volatile("s_branch " LBL(n) "\n\t.p2align 12\n" LBL(n) ":\n\ts_cbranch_execz " LBL(next))
#define HOP(v, n) "s_cmp_eq_u32 %0, " #v "\n\ts_cbranch_scc1 " LBL(n) "\n\t"
#define TOUR(sel, hops, ret) asm volatile("s_mov_b64 exec, 0\n\t" hops LBL(ret) ":\n\ts_mov_b64 exec, -1" :: "s"(sel) : "scc")


#define PSET(a, b) "buffer_load_dwordx4 v[" #a ":" #a "+3], %9, %10, 0 offen sc1\n\tbuffer_load_dwordx4 v[" #b ":" #b "+3], %9, %10, 0 offen offset:1024 sc1\n\t"
#define PCHK(a, b) "s_waitcnt vmcnt(2)\n\tv_mov_b32 v248, v[" #a "]\n\tv_mov_b32 v249, v[" #a "+2]\n\tv_mov_b32 v250, v[" #b "]\n\tv_mov_b32 v251, v[" #b "+2]\n\t" \
  "v_xor_b32 v[" #a "], %12, v[" #a "+1]\n\tds_write_b128 %11, v[248:251]\n\tv_bitop3_b32 v[" #a "], v[" #a "+3], %12, v[" #a "] bitop3:0xbe\n\t" \
  "v_bitop3_b32 v[" #a "], v[" #b "+1], %12, v[" #a "] bitop3:0xbe\n\tv_bitop3_b32 v[" #a "], v[" #b "+3], %12, v[" #a "] bitop3:0xbe\n\t" \
  "v_cmp_ne_u32 vcc, 0, v[" #a "]\n\ts_cbranch_vccz 2f\n\t"
#define PUNP(lo, hi, r) "v_lshlrev_b32 %" #lo ", 16, v" #r "\n\tv_and_b32 %" #hi ", 0xffff0000, v" #r "\n\t"
__device__ __forceinline__ void ppoll(const v4u rs, int hv, int hl, unsigned tag, v2f (&val)[4], int& spins) {
  int n = SPIN_CAP;
  float e[8];
  asm volatile("s_waitcnt vmcnt(0)\n\t" PSET(232, 236) "s_sleep %13\n\t" PSET(240, 244)
               "1:\n\t" PCHK(232, 236) PSET(232, 236) PCHK(240, 244) PSET(240, 244)
               "s_sub_u32 %0, %0, 1\n\ts_cbranch_scc0 1b\n"
               "2:\n\t" PUNP(1, 2, 248) PUNP(3, 4, 249) PUNP(5, 6, 250) PUNP(7, 8, 251) "s_waitcnt lgkmcnt(0)\n\ts_barrier"
               : "+s"(n), "=v"(e[0]), "=v"(e[1]), "=v"(e[2]), "=v"(e[3]), "=v"(e[4]), "=v"(e[5]), "=v"(e[6]), "=v"(e[7])
               : "v"(hv), "s"(rs), "v"(hl), "s"(tag), "n"(STG)
               : "memory", "vcc", "scc", "v232", "v233", "v234", "v235", "v236", "v237", "v238", "v239", "v240", "v241", "v242", "v243", "v244", "v245",
                 "v246", "v247", "v248", "v249", "v250", "v251");
  spins = SPIN_CAP - n;
#pragma unroll
  for (int j = 0; j < 4; ++j) val[j] = v2f{e[2 * j], e[2 * j + 1]};
}
template <int NP> __device__ __forceinline__ v2u gather16(const __amdgpu_buffer_rsrc_t rws, int so, int lane, unsigned tag, v2u init, unsigned gate, int& spins) {
  const unsigned one = (lane & 1) ? 0x3f800000u : 0x3f80u, s0 = (lane & 2) ? 0u : one, s1 = (lane & 2) ? one : 0u;
  v4u sel = {s0, s0, s1, s1};
  const bool in = (lane & 12) == 0, on = lane < (NP - 48) * 4;
  v4f c0 = {in ? blo(init[0]) : 0.f, in ? bhi(init[0]) : 0.f, in ? blo(init[1]) : 0.f, in ? bhi(init[1]) : 0.f};
  int vz = (lane >> 3) * 16384 + (lane & 7) * 16, vl = on ? vz : WS_BYTES;
  unsigned tl = on ? tag : 0u;
  asm volatile("" : "+v"(sel), "+v"(c0), "+v"(vz), "+v"(vl), "+v"(tl));
  if (gate) until<1>(gate);
  if (NP == NG) { SB(); PAD(12, 102); SB(); }
  v4f d;
  int spin = 0;
#pragma unroll 1
  for (; spin < SPIN_CAP; ++spin) {
    asm volatile("" ::: "memory");
    v4u z[4];
#pragma unroll
    for (int j = 0; j < 4; ++j) z[j] = LD128(rws, j < 3 ? vz : vl, so + j * 131072, 16);
    SB();
    unsigned bad = xo(z[0][1], tag, z[0][3] ^ tag);
    SB();
    d = MFMA(sel, (v4u{z[0][0], z[1][0], z[0][2], z[1][2]}), c0);
    SB();
    bad = xo(z[2][1], tag, xo(z[2][3], tag, bad));
    SB();
    d = MFMA(sel, (v4u{z[2][0], z[3][0], z[2][2], z[3][2]}), d);
    SB();
    bad = xo(z[1][1], tag, xo(z[1][3], tag, xo(z[3][1], tl, xo(z[3][3], tl, bad))));
    asm volatile("" : "+v"(d));
    if (NONE(bad != 0)) break;
  }
  spins = spin;
  float e[4];
#pragma unroll
  for (int k = 0; k < 4; ++k) { e[k] = d[k] + dpp<ROR8>(d[k]); asm volatile("" : "+v"(e[k])); }
#pragma unroll
  for (int k = 0; k < 4; ++k) { e[k] += dpp<ROR4>(e[k]); asm volatile("" : "+v"(e[k])); }
  return v2u{pk(e[0], e[1]), pk(e[2], e[3])};
}

constexpr int LSTEP = 4096;
template <bool RE>
__device__ __forceinline__ void prock(v4u (&a)[8], const v4u q, float& mx, LAS char* dst, bool lo8, bool q1,
                                      const __amdgpu_buffer_rsrc_t rn, int vo, int so) {
  float x[8];
#pragma unroll
  for (int i = 0; i < 8; ++i) {
    x[i] = dot2(a[i][3], q[3], dot2(a[i][2], q[2], dot2(a[i][1], q[1], dot2(a[i][0], q[0], 0.f))));
    asm volatile("" : "+v"(x[i]));
    SB();
    if (RE && i) { __builtin_amdgcn_s_sleep(KSL); SB(); }
    if (RE) a[i] = LD128(rn, vo + (i & 3) * 1024, so + (i >> 2) * LSTEP, KAUX);
    SB();
  }
  float y[4], z[2];
#pragma unroll
  for (int j = 0; j < 4; ++j) { const float u = x[j] + dpp<ROR8>(x[j]), v = x[j + 4] + dpp<ROR8>(x[j + 4]); y[j] = lo8 ? u : v; }
#pragma unroll
  for (int j = 0; j < 2; ++j) { const float u = y[j] + dpp<HMIR>(y[j]), v = y[j + 2] + dpp<HMIR>(y[j + 2]); z[j] = q1 ? v : u; z[j] += dpp<XOR1>(z[j]); z[j] += dpp<XOR2>(z[j]); }
  const unsigned r01 = pk(z[0], z[1]);
  const float SC = 0.08838834764831845f;
  const unsigned t01 = pk(blo(r01) * SC, bhi(r01) * SC);
  const v2f s = {blo(t01), bhi(t01)};
  mx = fmaxf(fmaxf(s[0], s[1]), mx);
  *(LAS v2f*)dst = s;
  SB();
}
template <bool RE>
__device__ __forceinline__ void procv(v4u (&a)[8], const LAS char* src, float (&acc)[8], const __amdgpu_buffer_rsrc_t rn, int vo, int so) {
  const v4f p0 = *(const LAS v4f*)src, p1 = *(const LAS v4f*)(src + 16);
  SB();
#pragma unroll
  for (int i = 0; i < 8; ++i) {
    const float p = i < 4 ? p0[i & 3] : p1[i & 3];
#pragma unroll
    for (int k = 0; k < 4; ++k) { acc[2 * k] = fmaf(p, blo(a[i][k]), acc[2 * k]); acc[2 * k + 1] = fmaf(p, bhi(a[i][k]), acc[2 * k + 1]); }
    asm volatile("" : "+v"(acc[0]), "+v"(acc[1]), "+v"(acc[2]), "+v"(acc[3]), "+v"(acc[4]), "+v"(acc[5]), "+v"(acc[6]), "+v"(acc[7]));
    SB();
    if (RE) a[i] = LD128(rn, vo + (i & 3) * 1024, so + (i >> 2) * LSTEP, KAUX);
    SB();
  }
}

__device__ __forceinline__ v4u tcol(const v4u* t, int f) {
  const unsigned sel = (f & 1) ? 0x07060302u : 0x05040100u;
  v4u o;
#pragma unroll
  for (int j = 0; j < 4; ++j) o[j] = __builtin_amdgcn_perm(t[2 * j + 1][f >> 1], t[2 * j][f >> 1], sel);
  return o;
}

__global__ void __launch_bounds__(512, 2) __attribute__((aligned(4096)))
fused_block(const u16* __restrict__ x, const u16* __restrict__ kc, const u16* __restrict__ vc, const u16* __restrict__ wq,
            const u16* __restrict__ wo, char* ws, u16* out, const u16* __restrict__ wg, const u16* __restrict__ wu,
            const u16* __restrict__ wd) {
  __shared__ __attribute__((aligned(16))) char smem_[L_END];
  LAS char* const sm = (LAS char*)smem_;
  const int tid = threadIdx.x, lane = tid & 63;
  LDSV(L_TAG + (tid & 124)) = 0u;
  const unsigned long long tl = __builtin_amdgcn_s_memrealtime();
  SB();
  int w = __builtin_amdgcn_readfirstlane(tid >> 6);
  const int r = lane >> 4, c = lane & 15;
  const int bid = blockIdx.x, xg = (bid >> 1) & 3, slot = bid >> 3;
  const int xc = bid & 7, side = slot & 1, pr = slot >> 1;
  const int b = pr >> 2, bh = pr * 8 + xc, h = bh & 31;
  const bool isRED = bid & 1, isGU = !isRED && pr < NG / 4 && !side;
  const int fg = xg * (NG / 4) + pr;
  unsigned tag;
  const int vo = lane * 16;
  const int sb = bh * (2 << 20) + (side * 4 + w - 4) * 8192;
  const int vx = (c >> 2) * 1024 + (r * 4 + (c & 3)) * 64, va = (c >> 2) * 1024 + (c & 1) * 64 + r * 16;
  const int vq = ((c >> 3) * 32 + r * 8) * 8192 + (c & 7) * 16, sq = b * (128 * 8192) + h * 256 + side * 128;
  asm volatile("" :: "s"(sb), "v"(vo), "v"(vx), "v"(va), "v"(vq), "s"(sq));
  bar();
  TOUR(w, HOP(1, 1) HOP(2, 3) HOP(3, 4), 100);
  asm volatile("" :: "s"(tl));
  const unsigned t0 = (unsigned)tl;
  asm volatile("" : "+s"(ws) : "s"(x), "s"(kc), "s"(vc), "s"(wq), "s"(wo));
  const __amdgpu_buffer_rsrc_t rws = RSRC(ws, WS_BYTES);
  const v4u rsw = {(unsigned)(uintptr_t)ws, (unsigned)((uintptr_t)ws >> 32), (unsigned)WS_BYTES, 0x00020000u};
  const __amdgpu_buffer_rsrc_t rk = RSRC(kc, 0x10000000);
#define SO(i) (sb + (((i) + bh) & 31) * 65536)
  v4u P[PRE + 1][8];
  if (w >= 4) {
#pragma unroll
    for (int i = 0; i < 8; ++i) {
      if (KS && i == KN) { SB(); __builtin_amdgcn_s_sleep(KS); SB(); }
      P[0][i] = LD128(rk, vo + (i & 3) * 1024, SO(0) + (i >> 2) * LSTEP, KAUX);
    }
#pragma unroll
    for (int u = 1; u <= PRE; ++u) {
#pragma unroll
      for (int i = 0; i < 8; ++i) {
        SB(); asm volatile("s_waitcnt vmcnt(%0)" :: "n"(PACE - 1)); SB();
        P[u][i] = LD128(rk, vo + (i & 3) * 1024, SO(u) + (i >> 2) * LSTEP, KAUX);
      }
    }
    SB();
  }
  asm volatile("" : "+s"(w));
  if (w == 0) {
    const __amdgpu_buffer_rsrc_t rq = RSRC(wq, D * 4096 * 2);
    const __amdgpu_buffer_rsrc_t rx = RSRC(x, 4096);
    v4u (&W0)[8] = P[0], (&W1)[8] = P[1];
    v4u* const xs = &P[2][0];
    v4u* const xa = &P[2][4];
#pragma unroll
    for (int i = 0; i < 4; ++i) xs[i] = LD128(rx, vx + i * 16, 0, 0);
#pragma unroll
    for (int i = 0; i < 2; ++i) xa[i] = LD128(rx, va + i * 128, b * 256, 0);
    const unsigned ev = __builtin_amdgcn_raw_buffer_load_b32(rws, 0, 0, 16);
    SB();
    PAD(1, 2);
    SB();
#pragma unroll
    for (int i = 0; i < 16; ++i) P[i >> 3][i & 7] = LD128(rq, vq, sq + ((i >> 3) * 64 + (i & 7)) * 8192, 2);
    SB();
    float s0 = 0.f, s1 = 0.f;
#pragma unroll
    for (int i = 0; i < 4; ++i) { s0 = dot2(xs[i][0], 0x3f803f80u, s0); s1 = dot2(xs[i][1], 0x3f803f80u, s1); s0 = dot2(xs[i][2], 0x3f803f80u, s0); s1 = dot2(xs[i][3], 0x3f803f80u, s1); }
    float sx = s0 + s1; sx += dpp<XOR1>(sx); sx += dpp<XOR2>(sx); sx = xrow_sum(sx);
    const float mean = sx * (1.f / 512.f);
    float v0 = 0.f, v1 = 0.f;
#pragma unroll
    for (int i = 0; i < 4; ++i)
#pragma unroll
      for (int k = 0; k < 4; ++k) { const float d0 = blo(xs[i][k]) - mean, d1 = bhi(xs[i][k]) - mean; v0 = fmaf(d0, d0, v0); v1 = fmaf(d1, d1, v1); }
    SB();
    PAD(2, 18);
    SB();
    float vs = v0 + v1; vs += dpp<XOR1>(vs); vs += dpp<XOR2>(vs); vs = xrow_sum(vs);
    const v2f st = ln_fin(mean, vs * (1.f / 512.f));
#pragma unroll
    for (int i = 0; i < 2; ++i)
#pragma unroll
      for (int k = 0; k < 4; ++k) xa[i][k] = ln_apply(xa[i][k], st);
    tag = __builtin_amdgcn_readfirstlane(ev) + 1u;
    v4f qa[8];
#pragma unroll
    for (int f = 0; f < 8; ++f) qa[f] = MFMA(xa[0], tcol(W0, f), (v4f{0.f, 0.f, 0.f, 0.f}));
    SB();
    PAD(3, 5);
    SB();
#pragma unroll
    for (int f = 0; f < 8; ++f) qa[f] = MFMA(xa[1], tcol(W1, f), qa[f]);
    float qv[4];
#pragma unroll
    for (int f = 0; f < 4; ++f) qv[f] = (c < 8 ? qa[f][0] : qa[f + 4][1]) + dpp<ROR8>(c < 8 ? qa[f + 4][0] : qa[f][1]);
#pragma unroll
    for (int t = 0; t < 2; ++t)
      __builtin_amdgcn_raw_buffer_store_b128(v4u{asu(qv[2 * t]), tag, asu(qv[2 * t + 1]), tag}, rws, r * 4096 + c * 16 + t * 256, WS_Q + h * 16384 + b * 1024 + side * 512, 16);
  }
  asm volatile("" : "+s"(w));

  if (w >= 4) {
    PADA(4, 6);
    const int sw = w - 4;
    const __amdgpu_buffer_rsrc_t rv = RSRC(vc, 0x10000000);
    v4u q;
#pragma unroll 1
    for (int spin = 0; spin < 64 * SPIN_CAP; ++spin) {
      const unsigned f = LDSV(L_FLG);
      q = *(volatile LAS v4u*)(sm + L_QP + c * 16);
      tag = LDSV(L_TAG);
      if (__builtin_amdgcn_readfirstlane(f)) break;
      __builtin_amdgcn_s_sleep(1);
    }
    LAS char* const sc = sm + L_SC + sw * 4096;
    constexpr int nu = NIT / 2;
    LAS char* const sd = sc + r * 32 + (c >> 2) * 8;
    const bool lo8 = c < 8, q1 = c & 4;
    float mx = -3.0e38f;
    prock<true>(P[0], q, mx, sd, lo8, q1, rk, vo, SO(PRE + 1));
    PAD(5, 7);
    prock<false>(P[1], q, mx, sd + 128, lo8, q1, rk, vo, 0);
    prock<false>(P[2], q, mx, sd + 256, lo8, q1, rk, vo, 0);
    PAD(6, 17);
    prock<false>(P[3], q, mx, sd + 384, lo8, q1, rk, vo, 0);
#pragma unroll
    for (int u = 4; u <= PRE; ++u) prock<false>(P[u], q, mx, sd + u * 128, lo8, q1, rk, vo, 0);
    PAD(7, 100);
    const int stg = (sw * 32 + slot) * SG >> 7;
    int tau = 0, due = 0;
    unsigned tev = 0, tc = 0;
    auto pace = [&](int it) __attribute__((always_inline)) {
      int now = (int)(clk() - t0);
      LDSV(L_LAG + sw * 4) = now - it * TN;
      LDSV(L_LAG + 16 + sw * 4) = now - ((due - tau) >> 6);
      const unsigned ta = __builtin_amdgcn_readfirstlane(tev);
      tev = LDSV(L_TE);
      if (ta != tc) {
        due = max(due, now * 64);
        tau = min(max(__builtin_amdgcn_readfirstlane((int)((float)(((int)(ta - t0) - stg) * 64 - due) * __builtin_amdgcn_rcpf((float)(2 * nu - it)))), 1), 64 * 250);
        tc = ta;
      }
#pragma unroll 1
      for (int k = 0; k < 200; ++k) {
        if (now * 64 >= due) break;
        __builtin_amdgcn_s_sleep(1);
        now = (int)(clk() - t0);
      }
      due += tau;
    };
#pragma unroll 1
    for (int it = PRE + 2; it <= nu; ++it) {
      pace(it);
      prock<true>(P[0], q, mx, sd + (it - 1) * 128, lo8, q1, RSRC(it < nu ? kc : vc, 0x10000000), vo, SO(it));
      PAD(19, 100);
    }
    {
      SB();
      PADA(8, 9);
      SB();
      mx = fmaxf(mx, dpp<HMIR>(mx)); mx = fmaxf(mx, dpp<ROR8>(mx)); mx = xrow_max(mx);
      const float mL = mx * L2E;
      float l = 0.f;
#pragma unroll
      for (int j = 0; j < 4; ++j) {
        v4f s = *(LAS v4f*)(sc + j * 1024 + lane * 16);
#pragma unroll
        for (int k = 0; k < 4; ++k) s[k] = __builtin_amdgcn_exp2f(fmaf(s[k], L2E, -mL));
        l += (s[0] + s[1]) + (s[2] + s[3]);
        *(LAS v4f*)(sc + j * 1024 + lane * 16) = s;
      }
      l = xrow_sum(row_sum(l));
      if (lane == 0) { *(volatile LAS v2f*)(sm + L_ML + sw * 8) = v2f{mx, l}; LDSADD(L_CNT, 1); }
      asm volatile("" ::: "memory");
    }
    PAD(9, 10);
    const int col = xg * D + (slot >> 1) * 32 + (slot & 1) * 4 + (lane & 3) * 8;
    const v2u xv0 = *(const v2u*)(x + col), xv1 = *(const v2u*)(x + (col ^ (2 * D)));
    float acc[8] = {0.f, 0.f, 0.f, 0.f, 0.f, 0.f, 0.f, 0.f};
#pragma unroll 1
    for (int it = 1; it < nu; ++it) {
      pace(nu + it);
      procv<true>(P[0], sc + r * 32 + (it - 1) * 128, acc, rv, vo, SO(it));
    }
    PAD(10, 11);
    procv<false>(P[0], sc + r * 32 + (nu - 1) * 128, acc, rv, vo, 0);
    { const v2f o2 = tr_reduce(acc); *(LAS unsigned*)(sm + L_PART + sw * 272 + ((c * 16 + r * 4) ^ WM * 2)) = pk(o2[0], o2[1]); }
    bar();
    tag = __builtin_amdgcn_readfirstlane(tag);
    if (isRED) {
      if (sw != 0) return;
      PADA(11, 12);
      const unsigned te = __builtin_amdgcn_readfirstlane(LDSV(L_TE));
      const int par = te & 2u, rb = xg ^ par, cg = slot;
      const v2u xv = par ? xv1 : xv0;
      int hv = (lane >> 2) * 8192 + (lane & 3) * 16, ov = lane < 4 ? (col ^ (par * D)) * 2 : 4096, hs = WS_H + rb * 2048 + cg * 64, spin;
      unsigned tv = tag;
      asm volatile("" : "+v"(hv), "+v"(tv), "+v"(ov), "+s"(hs));
      const v2u hp = gather16<64>(rws, WS_Z + rb * 4096 + cg * 128, lane, tag + 1u, xv, te + GA, spin);
      __builtin_amdgcn_raw_buffer_store_b128(v4u{hp[0], tv, hp[1], tv}, rws, hv, hs, 16);
      SB();
      const unsigned tha = clk();
      const v2u dn = gather16<NG>(rws, WS_O + rb * 4096 + cg * 128, lane, tag + 2u, hp, (tha + GC) | 1u, spin);
      __builtin_amdgcn_raw_buffer_store_b64(dn, RSRC(out, 4096), ov, 0, 0);
      SB();
      if (bid == 1 && lane == 0) __builtin_amdgcn_raw_buffer_store_b32(tag + 2u, rws, 0, 0, 16);
      __builtin_amdgcn_endpgm();
    }
  }
  asm volatile("" : "+s"(w));
  if (w < 4) {
    const int hw = w;
    PAD(18, 19);
    if (hw == 0) {
      v4u g[4];
      const int qo = WS_Q + h * 16384 + b * 4096;
      until<1>(t0 + max((int)(clk() - t0) + QD, QG));
#pragma unroll 1
      for (int spin = 0; spin < SPIN_CAP; ++spin) {
        asm volatile("" ::: "memory");
#pragma unroll
        for (int j = 0; j < 4; ++j) g[j] = LD128(rws, lane * 16 + j * 1024, qo, 16);
        unsigned bad = 0;
#pragma unroll
        for (int j = 0; j < 4; ++j) bad |= (g[j][1] ^ tag) | (g[j][3] ^ tag);
        if (__all(bad == 0)) break;
      }
      const float q0 = (asf(g[0][0]) + asf(g[1][0])) + (asf(g[2][0]) + asf(g[3][0]));
      const float q1 = (asf(g[0][2]) + asf(g[1][2])) + (asf(g[2][2]) + asf(g[3][2]));
      *(LAS unsigned*)(sm + L_QP + ((r >> 1) * 32 + (c & 7) * 4 + (c >> 3) * 2 + (r & 1)) * 4) = pk(q0, q1);
      LDSV(L_TAG) = tag;
      asm volatile("s_waitcnt lgkmcnt(0)" ::: "memory");
      LDSV(L_FLG) = 1u;
    }
    if (hw == 3 && (slot & 3) == 3) {
      const int o = WS_PR + (slot >> 2) * 16384 + xc * 1024 + (xc >> 2) * 8192;
      unsigned long long a0, a1, a2, a3, a4;
      unsigned g;
#define PQ(t, off) "s_load_dword %5, %6, " off "\n\ts_waitcnt lgkmcnt(0)\n\ts_memrealtime %" #t "\n\ts_waitcnt lgkmcnt(0)\n\t"
      asm volatile(PQ(0, "%7 offset:0x10200") PQ(1, "%7") PQ(2, "%8") PQ(3, "%8 offset:0x20100") PQ(4, "%7 offset:0x20100")
                   : "=&s"(a0), "=&s"(a1), "=&s"(a2), "=&s"(a3), "=&s"(a4), "=&s"(g) : "s"(ws), "s"(o), "s"(o ^ 8192));
      const int d1 = (int)(a1 - a0), d2 = (int)(a2 - a1), d3 = (int)(a3 - a2), d4 = (int)(a4 - a3);
      LDSV(L_PA) = (d1 < d2 ? 1 : -1) + (d4 < d3 ? 1 : -1);
    }
#pragma unroll 1
    for (int spin = 0; spin < 16 * SPIN_CAP; ++spin) { if (__builtin_amdgcn_readfirstlane(LDSV(L_FLG))) break; __builtin_amdgcn_s_sleep(4); }
    asm volatile("" ::: "memory");
    tag = __builtin_amdgcn_readfirstlane(*(LAS unsigned*)(sm + L_TAG));
    unsigned par = 0;
    float te = 0.f;
    auto round = [&](int s, int tr) __attribute__((always_inline)) {
      until<8>(t0 + tr);
      const v4u l2 = *(volatile LAS v4u*)(sm + L_LAG + (s & 2) * 8);
      const unsigned i2 = l2[0] + l2[1] + l2[2] + l2[3];
      if (lane < 8) __builtin_amdgcn_raw_buffer_store_b128(v4u{i2 + 4u * t0, tag, s ? i2 : LDSV(L_PA), tag}, rws, lane * 16, WS_PG + s * 32768 + bid * 128, 16);
      if (bid == 254) {
        until<4>(t0 + tr + DC);
        float n, si, sd;
#pragma unroll 1
        for (int k = 0; k < 4; ++k) {
          asm volatile("" ::: "memory");
          v4u g[4];
#pragma unroll
          for (int i = 0; i < 4; ++i) g[i] = LD128(rws, lane * 128, WS_PG + s * 32768 + i * 8192, 16);
          n = si = sd = 0.f;
#pragma unroll
          for (int i = 0; i < 4; ++i) if (g[i][1] == tag && g[i][3] == tag) { n += 1.f; si += (float)(int)(g[i][0] - 4u * t0); sd += (float)(int)g[i][2]; }
          n = xrow_sum(row_sum(n));
          if (__builtin_amdgcn_readfirstlane(asu(n)) == asu(256.f)) break;
        }
        const float rn = 0.25f * __builtin_amdgcn_rcpf(fmaxf(n, 1.f));
        si = xrow_sum(row_sum(si)) * rn + (float)(NIT * TN);
        sd = xrow_sum(row_sum(sd));
        if (s == 0) { par = sd < VTH ? 2u : 0u; te = si + (float)MA; }
        else { const float u = fmaxf(sd * rn - (float)LZ, 0.f); te += fminf(fminf(u * u * (KF * 0.01f), fmaf(u, KL * 0.1f, (float)KB)), (float)CX) - fminf(fmaxf(((float)LN - sd * rn) * (NK * 0.1f), 0.f), (float)NX); }
        te = fminf(fmaxf(te, 0.f), 40000.f);
        if (lane < 32) __builtin_amdgcn_raw_buffer_store_b64(v2u{((t0 + (unsigned)(int)te) & ~3u) | 1u | par, tag}, rws, lane * 256, WS_TE + s * 8192, 16);
      }
      until<4>(t0 + tr + DP);
#pragma unroll 1
      for (int k = s ? SPIN_CAP - 6 : 0; k < SPIN_CAP; ++k) {
        asm volatile("" ::: "memory");
        const v2u g = __builtin_amdgcn_raw_buffer_load_b64(rws, 0, WS_TE + s * 8192 + slot * 256, 16);
        if (__builtin_amdgcn_readfirstlane(g[1]) == tag) {
          LDSV(L_TE) = g[0];
          break;
        }
        __builtin_amdgcn_s_sleep(8);
      }
    };
    if (hw == 3) round(0, TA);
    v4u WO[4][8];
    {
      const __amdgpu_buffer_rsrc_t ro = RSRC(wo, D * 4096 * 2);
      const int vo = r * 8192 + c * 16, so = h * 131072 + hw * 256;
#pragma unroll
      for (int u = 0; u < 8; ++u) {
        v4u t[4];
#pragma unroll
        for (int j = 0; j < 4; ++j) {
          if (j == 2) { SB(); asm volatile("s_cmp_eq_u32 %0, 3\n\ts_cbranch_scc1 1f\n\ts_waitcnt vmcnt(0)\n1:" :: "s"(hw) : "scc"); SB(); }
          t[j] = LD128(ro, vo + (j & 1) * 1024, so + (((u >> 1) * 32768 + (u & 1) * 4096 + (j & 2) * 1024) ^ WM * 1024), OAUX);
        }
#pragma unroll
        for (int j = 0; j < 2; ++j)
#pragma unroll
          for (int f = 0; f < 8; ++f) {
            unsigned p = __builtin_amdgcn_perm(t[2 * j + 1][f >> 1], t[2 * j][f >> 1], (f & 1) ? 0x07060302u : 0x05040100u);
            asm volatile("" : "+v"(p));
            WO[u >> 1][f][(u & 1) * 2 + j] = p;
          }
        SB();
        if (u == 0) PAD(17, 100);
      }
    }
    TOUR(w, HOP(0, 8) HOP(1, 21), 102);
    if (isGU && hw < 2) {
      const int oc = lane & 3, rho = (lane >> 2) & 7, rd0 = lane >> 5;
      const __amdgpu_buffer_rsrc_t rg = RSRC(hw ? wu : wg, D * F * 2);
      const int go = ((rd0 * 32 + (rho & 3) * 8 + (rho & 4)) * F + oc * 8) * 2;
#pragma unroll 1
      for (int k = 0; k < 4; ++k) {
        until<8>(t0 + FW0 + k * FWD + (pr >> 1) * (FWD / 7));
        const int gs = (k * 64 * F + fg * 32) * 2;
        v4u t[8];
#pragma unroll
        for (int j = 0; j < 8; ++j) t[j] = LD128(rg, go, gs + ((j & 3) + (j >> 2) * 256) * F * 2, GAUX);
        const int kap = k * 16 + rd0 * 8 + rho;
        LAS char* dst = sm + L_FR + oc * FRT + ((kap >> 2) * 64 + (kap & 3) * 16 + hw) * 16;
#pragma unroll
        for (int f = 0; f < 8; ++f) *(LAS v4u*)(dst + f * 32) = tcol(t, f);
      }
      const __amdgpu_buffer_rsrc_t rd = RSRC(wd, D * F * 2);
#pragma unroll 1
      for (int k = 0; k < 2; ++k) {
        until<8>(t0 + FW0 + (k + 4) * FWD + (pr >> 1) * (FWD / 7));
        v4u t[8];
#pragma unroll
        for (int j = 0; j < 8; ++j) t[j] = LD128(rd, r * 8192 + c * 16, (fg * 32 + j) * 1024 + (k * 2 + hw) * 256, DAUX);
#pragma unroll
        for (int f = 0; f < 8; ++f) *(LAS v4u*)(sm + L_FD + (k * 2 + hw) * 8192 + f * 1024 + lane * 16) = tcol(t, f);
      }
    }
    if (hw == 2) {
#pragma unroll 1
      for (int spin = 0; spin < SPIN_CAP; ++spin) { if (__builtin_amdgcn_readfirstlane(LDSV(L_CNT)) == 4) break; __builtin_amdgcn_s_sleep(8); }
      asm volatile("" ::: "memory");
      const v2f ml = *(volatile LAS v2f*)(sm + L_ML + (lane & 3) * 8);
      float M = fmaxf(ml[0], dpp<XOR1>(ml[0])); M = fmaxf(M, dpp<XOR2>(M));
      float L = ml[1] * __builtin_amdgcn_exp2f((ml[0] - M) * L2E); L += dpp<XOR1>(L); L += dpp<XOR2>(L);
      if (lane < 8) __builtin_amdgcn_raw_buffer_store_b128(v4u{asu(M), tag, asu(L), tag}, rws, lane * 16, WS_ML + bid * 128, 16);
      v4u g;
#pragma unroll 1
      for (int spin = 0; spin < SPIN_CAP; ++spin) {
        __builtin_amdgcn_s_sleep(64);
        asm volatile("" ::: "memory");
        g = LD128(rws, 0, WS_ML + (bid ^ 8) * 128, 16);
        if (__all(g[1] == tag && g[3] == tag)) break;
      }
      const float Mo = asf(g[0]), Mt = fmaxf(M, Mo);
      const float La = L * __builtin_amdgcn_exp2f((M - Mt) * L2E), Lb = asf(g[2]) * __builtin_amdgcn_exp2f((Mo - Mt) * L2E);
      if (lane < 4) *(LAS float*)(sm + L_FA + lane * 4) = __builtin_amdgcn_exp2f((ml[0] - Mt) * L2E) / (La + Lb);
    }
    if (hw == 3) round(2, T3);
    SB();
    PADA(21, 13);
    SB();
    {
      int zv = ((c >> 2) * 2 + r) * 128 + (c & 3) * 16 + (r < 2 ? 0 : WS_BYTES), pv = L_PART + (c & 3) * 272 + r * 16;
      const int zs = WS_Z + h * 16384 + b * 4096 + hw * 1024 + side * 64;
      unsigned tz = tag + 1u;
      asm volatile("" : "+v"(zv), "+v"(pv), "+v"(tz));
      bar();
      v4u pa[4];
#pragma unroll
      for (int s = 0; s < 4; ++s) pa[s] = *(LAS v4u*)(sm + pv + s * 64);
      const v4f fa = *(LAS v4f*)(sm + L_FA);
      SB();
      v4f a[8];
      float y[8];
      unsigned zp[4];
#pragma unroll
      for (int f = 0; f < 10; ++f) {
#pragma unroll
        for (int s = 0; s < 4; ++s) {
          const int e = f - 2 + (s >> 1), p = s & 1;
          if (f < 8) a[f] = MFMA(pa[s], WO[s][f], s ? a[f] : (v4f{0.f, 0.f, 0.f, 0.f}));
          SB();
          if (s < 2 && e >= 0) {
            y[e] = p ? fmaf(fa[3], a[e][3], fmaf(fa[2], a[e][2], y[e])) : fmaf(fa[1], a[e][1], fa[0] * a[e][0]);
            asm volatile("" : "+v"(y[e]));
          }
          if (s == 2 && f >= 3 && (f & 1)) {
            zp[f / 2 - 1] = pk(y[f - 3], y[f - 2]);
            asm volatile("" : "+v"(zp[f / 2 - 1]));
            if (f > 5) zp[f / 2 - 3] = (r & 1) ? zp[f / 2 - 1] : zp[f / 2 - 3];
          }
          SB();
        }
      }
      __builtin_amdgcn_raw_buffer_store_b128(v4u{zp[0], tz, zp[1], tz}, rws, zv, zs, 16);
    }
  }

  if (isGU) {
    SB();
    PAD(13, 14);
    SB();
    if (w >= 4) {
      v4u fd[8];
#pragma unroll
      for (int f = 0; f < 8; ++f) fd[f] = *(LAS v4u*)(sm + L_FD + (w - 4) * 8192 + f * 1024 + lane * 16);
#pragma unroll
      for (int f = 0; f < 8; ++f) asm volatile("" : "+v"(fd[f]));
      const int hv = WS_H + (fg & 14) * 8192 + ((xg ^ LDSV(L_TE)) & 2) * 4096 + (w - 4) * 2048 + lane * 16;
      int ha_ = L_HH + (c >> 2) * 64 + r * 16, ov = r * 4096 + (c >> 2) * 256 + (c & 3) * 16, os = WS_O + (fg >> 1) * 16384 + (w - 4) * 1024 + (fg & 1) * 64, spin;
      v4u o0 = {0u, tag + 2u, 0u, tag + 2u}, o1 = o0;
      int sv = L_ST + (w - 4) * 8;
      asm volatile("" : "+v"(ha_), "+v"(ov), "+s"(os), "+v"(o0), "+v"(o1), "+v"(sv));
      PAD(15, 16);
      v2f hv4[4];
      ppoll(rsw, hv, L_HN + (w - 4) * 1040 + lane * 16 + (int)(uintptr_t)sm, tag, hv4, spin);
      const v2f st = ln_stats(hv4);
      *(volatile LAS v2u*)(sm + sv) = v2u{asu(st[0]) | 1u, asu(st[1])};
      PAD(16, 102);
      bar();
      const v4u ha = *(LAS v4u*)(sm + ha_);
      v4f o[8];
#pragma unroll
      for (int f = 0; f < 8; ++f) {
        o[f] = MFMA(ha, fd[f], (v4f{0.f, 0.f, 0.f, 0.f}));
        SB();
        if (f == 4) o0[0] = pk(o[0][0], o[1][0]);
        if (f == 6) { o0[2] = pk(o[2][0], o[3][0]); __builtin_amdgcn_raw_buffer_store_b128(o0, rws, ov, os, 16); }
        if (f == 7) o1[0] = pk(o[4][0], o[5][0]);
        SB();
      }
      o1[2] = pk(o[6][0], o[7][0]);
      __builtin_amdgcn_raw_buffer_store_b128(o1, rws, ov + 128, os, 16);
    } else {
      v4u fr[16], hb[4];
#pragma unroll
      for (int s = 0; s < 16; ++s) fr[s] = *(LAS v4u*)(sm + L_FR + w * FRT + (s * 64 + lane) * 16);
      v4f cs = {0.f, 0.f, 0.f, 0.f};
#pragma unroll
      for (int s = 0; s < 16; ++s) cs = MFMA(fr[s], (v4u{0x3f803f80u, 0x3f803f80u, 0x3f803f80u, 0x3f803f80u}), cs);
      int sa = L_ST + (c & 3) * 8 + (int)(uintptr_t)sm, hw_ = c < 4 ? L_HH + c * 64 + w * 16 + r * 4 : L_SC + tid * 4;
      asm volatile("" : "+v"(cs), "+v"(sa), "+v"(hw_));
      PAD(14, 15);
      bar();
#pragma unroll
      for (int u = 0; u < 4; ++u) hb[u] = *(LAS v4u*)(sm + L_HN + (c & 3) * 1040 + (c >> 2) * 64 + r * 16 + u * 256);
      SB();
      v4f ac[4];
#pragma unroll
      for (int i = 0; i < 4; ++i) ac[i] = MFMA(fr[i], hb[0], (v4f{0.f, 0.f, 0.f, 0.f}));
#pragma unroll
      for (int s = 4; s < 16; ++s) ac[s & 3] = MFMA(fr[s], hb[s >> 2], ac[s & 3]);
      SB();
      v2u st;
      asm volatile("ds_read_b64 %0, %1" : "=v"(st) : "v"(sa) : "memory");
      SB();
      float acc[4];
#pragma unroll
      for (int k = 0; k < 4; ++k) { acc[k] = ac[0][k] + dpp<SHL + 4>(ac[1][k]); asm volatile("" : "+v"(acc[k])); }
#pragma unroll
      for (int k = 0; k < 4; ++k) { acc[k] += dpp<SHL + 8>(ac[2][k]); asm volatile("" : "+v"(acc[k])); }
#pragma unroll
      for (int k = 0; k < 4; ++k) { acc[k] += dpp<SHL + 12>(ac[3][k]); asm volatile("" : "+v"(acc[k])); }
      asm volatile("1:\n\ts_waitcnt lgkmcnt(0)\n\tv_cmp_eq_u64 vcc, 0, %0\n\ts_cbranch_vccz 2f\n\tds_read_b64 %0, %1\n\ts_branch 1b\n2:" : "+v"(st) : "v"(sa) : "memory", "vcc");
      const float mean = asf(st[0]), rs = asf(st[1]), kg = -L2E * rs;
#pragma unroll
      for (int k = 0; k < 4; ++k) { acc[k] = fmaf(-mean, cs[k], acc[k]) * ((k & 1) ? rs : kg); asm volatile("" : "+v"(acc[k])); }
      const float h0 = acc[1] * __builtin_amdgcn_rcpf(1.f + __builtin_amdgcn_exp2f(acc[0])), h1 = acc[3] * __builtin_amdgcn_rcpf(1.f + __builtin_amdgcn_exp2f(acc[2]));
      *(LAS unsigned*)(sm + hw_) = pk(h0, h1);
      bar();
    }
  }
}

extern "C" void kernel_launch(void* const* d_in, const int* in_sizes, int n_in, void* d_out, int out_size, void* d_ws, size_t ws_size, hipStream_t stream) {
  fused_block<<<256, 512, 0, stream>>>((const u16*)d_in[0], (const u16*)d_in[1], (const u16*)d_in[2], (const u16*)d_in[3], (const u16*)d_in[4],
                                       (char*)d_ws, (u16*)d_out, (const u16*)d_in[6], (const u16*)d_in[7], (const u16*)d_in[8]);
}
```
